```python
import torch

def _softmax(x, axis=-1):
    return torch.softmax(x, dim=axis)

def _gelu(x):
    return torch.nn.functional.gelu(x, approximate='tanh')

def _silu(x):
    return torch.nn.functional.silu(x)

def _sigmoid(x):
    return torch.sigmoid(x)

def _layernorm(x, axis=-1, eps=1e-05):
    m = x.mean(dim=axis, keepdim=True)
    v = x.var(dim=axis, keepdim=True, unbiased=False)
    return (x - m) / torch.sqrt(v + eps)

def _scatter_add(tgt, idx, src, axis=0):
    return tgt.clone().index_add_(axis, idx, src)

def _segment_sum(src, seg, n):
    out = torch.zeros((n,) + src.shape[1:], dtype=src.dtype)
    return out.index_add_(0, seg, src)

def _pad(x, pw):
    flat = tuple((v for pair in reversed(pw) for v in pair))
    return torch.nn.functional.pad(x, flat)
kernel_name = 'sequential_preLN__spectral_fnet__mixer_gmlp__prompt'

def setup_inputs():

    _w = lambda a: (lambda t: t.to(torch.bfloat16) if t.is_floating_point() else t)(torch.as_tensor(a))

    def _A_setup():
        B, S, D = (1, 2048, 640)
        ns = torch.arange(S)[:, None] * torch.arange(S)[None, :]
        nd = torch.arange(D)[:, None] * torch.arange(D)[None, :]
        Ws_re = torch.cos(-2.0 * torch.pi * ns / S).to(float)
        Ws_im = torch.sin(-2.0 * torch.pi * ns / S).to(float)
        Wd_re = torch.cos(-2.0 * torch.pi * nd / D).to(float)
        Wd_im = torch.sin(-2.0 * torch.pi * nd / D).to(float)
        return {'x': _w((lambda *s: torch.randn(*s, dtype=torch.bfloat16) * 0.05)(B, S, D)), 'dft_seq_re': _w(Ws_re), 'dft_seq_im': _w(Ws_im), 'dft_hid_re': _w(Wd_re), 'dft_hid_im': _w(Wd_im)}
    _A_in = _A_setup()

    def _B_setup():
        B, S, D, F = (1, 2048, 640, 1536)
        Fh = F // 2
        return {'x': _w((lambda *s: torch.randn(*s, dtype=torch.bfloat16) * 0.05)(B, S, D)), 'w_in': _w((lambda *s: torch.randn(*s, dtype=torch.bfloat16) * 0.8)(D, 2 * Fh)), 'w_sp': _w(0.01 * (lambda *s: torch.randn(*s, dtype=torch.bfloat16) * 0.8)(S, S)), 'b_sp': _w(1.0 + 0.01 * (lambda *s: torch.randn(*s, dtype=torch.bfloat16) * 0.8)(S)), 'w_out': _w((lambda *s: torch.randn(*s, dtype=torch.bfloat16) * 0.8)(Fh, D))}
    _B_in = _B_setup()
    return {'A_x': _A_in['x'], 'A_dft_seq_re': _A_in['dft_seq_re'], 'A_dft_seq_im': _A_in['dft_seq_im'], 'A_dft_hid_re': _A_in['dft_hid_re'], 'A_dft_hid_im': _A_in['dft_hid_im'], 'B_x': _B_in['x'], 'B_w_in': _B_in['w_in'], 'B_w_sp': _B_in['w_sp'], 'B_b_sp': _B_in['b_sp'], 'B_w_out': _B_in['w_out']}

def reference(A_x, A_dft_seq_re, A_dft_seq_im, A_dft_hid_re, A_dft_hid_im, B_x, B_w_in, B_w_sp, B_b_sp, B_w_out):

    def _A_ref(x, dft_seq_re, dft_seq_im, dft_hid_re, dft_hid_im):
        a = torch.einsum('bsd,ed->bse', x, dft_hid_re)
        b = torch.einsum('bsd,ed->bse', x, dft_hid_im)
        y = torch.einsum('ts,bse->bte', dft_seq_re, a) - torch.einsum('ts,bse->bte', dft_seq_im, b)
        return y

    def _B_ref(x, w_in, w_sp, b_sp, w_out):
        B, S, D = x.shape
        F = w_in.shape[-1]
        Fh = F // 2
        z = _gelu(torch.einsum('bsd,df->bsf', x, w_in))
        z1 = z[..., :Fh].clone()
        z2 = _layernorm(z[..., Fh:], axis=-1)
        g = torch.einsum('ts,bsf->btf', w_sp, z2) + b_sp[None, :, None]
        return torch.einsum('bsf,fd->bsd', z1 * g, w_out)
    x = A_x
    h = x + _A_ref(_layernorm(x, axis=-1), A_dft_seq_re, A_dft_seq_im, A_dft_hid_re, A_dft_hid_im)
    h2 = h + _B_ref(_layernorm(h, axis=-1), B_w_in, B_w_sp, B_b_sp, B_w_out)
    return h2
```

```cpp
#include <hip/hip_runtime.h>
#include <hip/hip_bf16.h>
typedef unsigned short u16;
__device__ __forceinline__ float b2f(u16 v){ return __uint_as_float(((unsigned)v)<<16); }
__device__ __forceinline__ u16 f2b(float f){ __hip_bfloat16 b = __float2bfloat16(f); return *(u16*)&b; }
constexpr int S=2048, D=640, F=1536, FH=768;

__global__ void ln_k(const u16* in, int ld_in, u16* out, int ld_out, int n){
  int row=blockIdx.x; __shared__ float red[256];
  float s=0; for(int c=threadIdx.x;c<n;c+=256) s+=b2f(in[row*ld_in+c]);
  red[threadIdx.x]=s; __syncthreads();
  for(int o=128;o;o>>=1){ if(threadIdx.x<o) red[threadIdx.x]+=red[threadIdx.x+o]; __syncthreads(); }
  float m=red[0]/n; __syncthreads();
  s=0; for(int c=threadIdx.x;c<n;c+=256){ float d=b2f(in[row*ld_in+c])-m; s+=d*d; }
  red[threadIdx.x]=s; __syncthreads();
  for(int o=128;o;o>>=1){ if(threadIdx.x<o) red[threadIdx.x]+=red[threadIdx.x+o]; __syncthreads(); }
  float r=rsqrtf(red[0]/n+1e-5f);
  for(int c=threadIdx.x;c<n;c+=256) out[row*ld_out+c]=f2b((b2f(in[row*ld_in+c])-m)*r);
}
__global__ void gemm_k(const u16* A,int sam,int sak,const u16* B,int sbk,int sbn,float* C,int N,int K,float alpha,int accum){
  __shared__ float As[16][17], Bs[16][17];
  int tx=threadIdx.x&15, ty=threadIdx.x>>4; int m=blockIdx.y*16+ty, n=blockIdx.x*16+tx; float acc=0;
  for(int k0=0;k0<K;k0+=16){
    As[ty][tx]=b2f(A[(long)m*sam+(long)(k0+tx)*sak]);
    Bs[ty][tx]=b2f(B[(long)(k0+ty)*sbk+(long)n*sbn]);
    __syncthreads();
    for(int k=0;k<16;k++) acc+=As[ty][k]*Bs[k][tx];
    __syncthreads();
  }
  float v=alpha*acc; if(accum) v+=C[(long)m*N+n]; C[(long)m*N+n]=v;
}
__global__ void cvt_k(const float* in,u16* out,int n){ int i=blockIdx.x*256+threadIdx.x; if(i<n) out[i]=f2b(in[i]); }
__global__ void addx_k(const float* y,const u16* x,u16* out,int n){ int i=blockIdx.x*256+threadIdx.x; if(i<n) out[i]=f2b(y[i]+b2f(x[i])); }
__global__ void gelu_k(const float* in,u16* out,int n){ int i=blockIdx.x*256+threadIdx.x; if(i<n){ float v=in[i]; float t=tanhf(0.7978845608028654f*(v+0.044715f*v*v*v)); out[i]=f2b(0.5f*v*(1.f+t)); } }
__global__ void gate_k(const float* g,const u16* z,const u16* bsp,u16* u){ int i=blockIdx.x*256+threadIdx.x; int t=i/FH,f=i%FH; u[i]=f2b(b2f(z[t*F+f])*(g[i]+b2f(bsp[t]))); }

extern "C" void kernel_launch(void* const* d_in, const int* in_sizes, int n_in, void* d_out, int out_size, void* d_ws, size_t ws_size, hipStream_t st){
  const u16* x=(const u16*)d_in[0]; const u16* wsr=(const u16*)d_in[1]; const u16* wsi=(const u16*)d_in[2];
  const u16* wdr=(const u16*)d_in[3]; const u16* wdi=(const u16*)d_in[4];
  const u16* win=(const u16*)d_in[6]; const u16* wsp=(const u16*)d_in[7]; const u16* bsp=(const u16*)d_in[8]; const u16* wout=(const u16*)d_in[9];
  char* w=(char*)d_ws; const size_t MB=1<<20;
  u16* xn=(u16*)(w); u16* a=(u16*)(w+8*MB); u16* b=(u16*)(w+16*MB); float* t=(float*)(w+24*MB); u16* h=(u16*)(w+48*MB);
  u16* hn=(u16*)(w+56*MB); u16* z=(u16*)(w+64*MB); u16* z2n=(u16*)(w+80*MB); u16* u=(u16*)(w+88*MB);
  ln_k<<<S,256,0,st>>>(x,D,xn,D,D);
  gemm_k<<<dim3(D/16,S/16),256,0,st>>>(xn,D,1,wdr,1,D,t,D,D,1.f,0); cvt_k<<<S*D/256,256,0,st>>>(t,a,S*D);
  gemm_k<<<dim3(D/16,S/16),256,0,st>>>(xn,D,1,wdi,1,D,t,D,D,1.f,0); cvt_k<<<S*D/256,256,0,st>>>(t,b,S*D);
  gemm_k<<<dim3(D/16,S/16),256,0,st>>>(wsr,S,1,a,D,1,t,D,S,1.f,0);
  gemm_k<<<dim3(D/16,S/16),256,0,st>>>(wsi,S,1,b,D,1,t,D,S,-1.f,1);
  addx_k<<<S*D/256,256,0,st>>>(t,x,h,S*D);
  ln_k<<<S,256,0,st>>>(h,D,hn,D,D);
  gemm_k<<<dim3(F/16,S/16),256,0,st>>>(hn,D,1,win,F,1,t,F,D,1.f,0); gelu_k<<<S*F/256,256,0,st>>>(t,z,S*F);
  ln_k<<<S,256,0,st>>>(z+FH,F,z2n,FH,FH);
  gemm_k<<<dim3(FH/16,S/16),256,0,st>>>(wsp,S,1,z2n,FH,1,t,FH,S,1.f,0);
  gate_k<<<S*FH/256,256,0,st>>>(t,z,bsp,u);
  gemm_k<<<dim3(D/16,S/16),256,0,st>>>(u,FH,1,wout,D,1,t,D,FH,1.f,0);
  addx_k<<<S*D/256,256,0,st>>>(t,h,(u16*)d_out,S*D);
}
```

```cpp
#include <hip/hip_runtime.h>
#include <hip/hip_bf16.h>
typedef unsigned short u16;
__device__ __forceinline__ float b2f(u16 v){ return __uint_as_float(((unsigned)v)<<16); }
__device__ __forceinline__ u16 f2b(float f){ __hip_bfloat16 b = __float2bfloat16(f); return *(u16*)&b; }
constexpr int S=2048, D=640, F=1536, FH=768;

__global__ void ln_k(const u16* in, int ld_in, u16* out, int ld_out, int n){
  int row=blockIdx.x; __shared__ float red[256];
  float s=0; for(int c=threadIdx.x;c<n;c+=256) s+=b2f(in[row*ld_in+c]);
  red[threadIdx.x]=s; __syncthreads();
  for(int o=128;o;o>>=1){ if(threadIdx.x<o) red[threadIdx.x]+=red[threadIdx.x+o]; __syncthreads(); }
  float m=red[0]/n; __syncthreads();
  s=0; for(int c=threadIdx.x;c<n;c+=256){ float d=b2f(in[row*ld_in+c])-m; s+=d*d; }
  red[threadIdx.x]=s; __syncthreads();
  for(int o=128;o;o>>=1){ if(threadIdx.x<o) red[threadIdx.x]+=red[threadIdx.x+o]; __syncthreads(); }
  float r=rsqrtf(red[0]/n+1e-5f);
  for(int c=threadIdx.x;c<n;c+=256) out[row*ld_out+c]=f2b((b2f(in[row*ld_in+c])-m)*r);
}
__global__ void gemm_k(const u16* A,int sam,int sak,const u16* B,int sbk,int sbn,float* C,int N,int K,float alpha,int accum){
  __shared__ float As[16][17], Bs[16][17];
  int tx=threadIdx.x&15, ty=threadIdx.x>>4; int m=blockIdx.y*16+ty, n=blockIdx.x*16+tx; float acc=0;
  for(int k0=0;k0<K;k0+=16){
    As[ty][tx]=b2f(A[(long)m*sam+(long)(k0+tx)*sak]);
    Bs[ty][tx]=b2f(B[(long)(k0+ty)*sbk+(long)n*sbn]);
    __syncthreads();
    for(int k=0;k<16;k++) acc+=As[ty][k]*Bs[k][tx];
    __syncthreads();
  }
  float v=alpha*acc; if(accum) v+=C[(long)m*N+n]; C[(long)m*N+n]=v;
}
__global__ void cvt_k(const float* in,u16* out,int n){ int i=blockIdx.x*256+threadIdx.x; if(i<n) out[i]=f2b(in[i]); }
__global__ void addx_k(const float* y,const u16* x,u16* out,int n){ int i=blockIdx.x*256+threadIdx.x; if(i<n) out[i]=f2b(y[i]+b2f(x[i])); }
__global__ void gelu_k(const float* in,u16* out,int n){ int i=blockIdx.x*256+threadIdx.x; if(i<n){ float v=in[i]; float t=tanhf(0.7978845608028654f*(v+0.044715f*v*v*v)); out[i]=f2b(0.5f*v*(1.f+t)); } }
__global__ void gate_k(const float* g,const u16* z,const u16* bsp,u16* u){ int i=blockIdx.x*256+threadIdx.x; int t=i/FH,f=i%FH; u[i]=f2b(b2f(z[t*F+f])*(g[i]+b2f(bsp[t]))); }


__global__ void probe_k(const char* p,int n,unsigned* sink){
  unsigned off=0, acc=0;
  for(int i=0;i<n;i++){ unsigned v=*(const volatile unsigned*)(p+off); asm volatile("v_and_b32 %0, 0, %0":"+v"(v)); off+=128+v; acc+=v; }
  if(acc==123456u) *sink=acc;
}
extern "C" void kernel_launch(void* const* d_in, const int* in_sizes, int n_in, void* d_out, int out_size, void* d_ws, size_t ws_size, hipStream_t st){
  const u16* x=(const u16*)d_in[0]; const u16* wsr=(const u16*)d_in[1]; const u16* wsi=(const u16*)d_in[2];
  const u16* wdr=(const u16*)d_in[3]; const u16* wdi=(const u16*)d_in[4];
  const u16* win=(const u16*)d_in[6]; const u16* wsp=(const u16*)d_in[7]; const u16* bsp=(const u16*)d_in[8]; const u16* wout=(const u16*)d_in[9];
  char* w=(char*)d_ws; const size_t MB=1<<20;
  u16* xn=(u16*)(w); u16* a=(u16*)(w+8*MB); u16* b=(u16*)(w+16*MB); float* t=(float*)(w+24*MB); u16* h=(u16*)(w+48*MB);
  u16* hn=(u16*)(w+56*MB); u16* z=(u16*)(w+64*MB); u16* z2n=(u16*)(w+80*MB); u16* u=(u16*)(w+88*MB);
  ln_k<<<S,256,0,st>>>(x,D,xn,D,D);
  gemm_k<<<dim3(D/16,S/16),256,0,st>>>(xn,D,1,wdr,1,D,t,D,D,1.f,0); cvt_k<<<S*D/256,256,0,st>>>(t,a,S*D);
  gemm_k<<<dim3(D/16,S/16),256,0,st>>>(xn,D,1,wdi,1,D,t,D,D,1.f,0); cvt_k<<<S*D/256,256,0,st>>>(t,b,S*D);
  gemm_k<<<dim3(D/16,S/16),256,0,st>>>(wsr,S,1,a,D,1,t,D,S,1.f,0);
  gemm_k<<<dim3(D/16,S/16),256,0,st>>>(wsi,S,1,b,D,1,t,D,S,-1.f,1);
  addx_k<<<S*D/256,256,0,st>>>(t,x,h,S*D);
  ln_k<<<S,256,0,st>>>(h,D,hn,D,D);
  gemm_k<<<dim3(F/16,S/16),256,0,st>>>(hn,D,1,win,F,1,t,F,D,1.f,0); gelu_k<<<S*F/256,256,0,st>>>(t,z,S*F);
  ln_k<<<S,256,0,st>>>(z+FH,F,z2n,FH,FH);
  gemm_k<<<dim3(FH/16,S/16),256,0,st>>>(wsp,S,1,z2n,FH,1,t,FH,S,1.f,0);
  gate_k<<<S*FH/256,256,0,st>>>(t,z,bsp,u);
  gemm_k<<<dim3(D/16,S/16),256,0,st>>>(u,FH,1,wout,D,1,t,D,FH,1.f,0);
  addx_k<<<S*D/256,256,0,st>>>(t,h,(u16*)d_out,S*D);
}
```

```cpp
#include <hip/hip_runtime.h>
#include <hip/hip_bf16.h>
#pragma clang diagnostic ignored "-Wint-to-pointer-cast"
namespace {
typedef unsigned short u16; typedef unsigned u32; typedef unsigned long long u64;
typedef u32 v4u __attribute__((ext_vector_type(4)));
typedef u32 v2u __attribute__((ext_vector_type(2)));
typedef float f4 __attribute__((ext_vector_type(4)));
typedef float f2 __attribute__((ext_vector_type(2)));
typedef __bf16 bf2 __attribute__((ext_vector_type(2)));
typedef short s4 __attribute__((ext_vector_type(4)));
#define LAS __attribute__((address_space(3)))
#define DI __device__ __forceinline__
constexpr u32 MB = 1u << 20;
constexpr u32 CELL = 16384, GRP_OFF = 1 * MB, XT_OFF = 2 * MB, ZT_OFF = 5 * MB, WI_OFF = 8 * MB, WO_OFF = 10 * MB, LOC_OFF = 12 * MB, WS_BYTES = 44 * MB;
constexpr u32 F0_OFF = GRP_OFF + 8 * CELL, FB_OFF = F0_OFF + 2048, PRB_OFF = GRP_OFF + 16 * CELL;
constexpr u32 H_IN = 5 * CELL, U_IN = 8 * CELL, ST2_IN = 12 * CELL, G3_IN = ST2_IN + 1024, F1_IN = ST2_IN + 3072, F2_IN = F1_IN + 128, F5_IN = F2_IN + 128;
constexpr int SPIN_CAP = 1 << 16;
constexpr float LN_EPS = 1e-5f;
#define SB __builtin_amdgcn_sched_barrier(0)
#define BAR() do { asm volatile("" ::: "memory"); __builtin_amdgcn_s_barrier(); asm volatile("" ::: "memory"); } while (0)
#define WAITV(n) asm volatile("s_waitcnt vmcnt(" #n ")" ::: "memory")
#define DRAIN() do { __builtin_amdgcn_s_waitcnt(0x0F70); WAITV(0); } while (0)
#define WAITL0 __builtin_amdgcn_s_waitcnt(0xC07F)
#define MFMA(acc, a, b) asm volatile("v_mfma_f32_16x16x32_bf16 %0, %1, %2, %0" : "+v"(acc) : "v"(a), "v"(b))
#define MFMA0(acc, a, b) asm volatile("v_mfma_f32_16x16x32_bf16 %0, %1, %2, 0" : "=&v"(acc) : "v"(a), "v"(b))
#define MFMA_END asm volatile("s_nop 7\n s_nop 0"); SB
#define WAITL(n) __builtin_amdgcn_s_waitcnt(0xC07F | ((n) << 8))
#define SKIP(n) do { SB; asm volatile("s_branch 1f\n .fill " #n ", 4, 0xBF800000\n1:"); SB; } while (0)
#define LDS128(base, off) (*(const LAS v4u*)((base) + (off)))
#define LDG(v, s) __builtin_amdgcn_raw_buffer_load_b128(rw, v, s, 16)
#define STG(d, v, s) __builtin_amdgcn_raw_buffer_store_b128(d, rw, (v) + (s), 0, 16)
DI v4u srd(const void* p, u32 bytes) { const u64 a = (u64)p; return v4u{(u32)a, (u32)(a >> 32) & 0xffffu, bytes, 0x00020000u}; }
DI u32 pk(float lo, float hi) { return __builtin_bit_cast(u32, __builtin_convertvector(f2{lo, hi}, bf2)); }
DI float F_(u32 v) { return __builtin_bit_cast(float, v); }
DI float blo(u32 v) { return __builtin_bit_cast(float, v << 16); }
DI float bhi(u32 v) { return __builtin_bit_cast(float, v & 0xffff0000u); }
DI float sum_q(float v) {
    auto a = __builtin_amdgcn_permlane16_swap(__builtin_bit_cast(u32, v), __builtin_bit_cast(u32, v), false, false);
    v = __builtin_bit_cast(float, (u32)a[0]) + __builtin_bit_cast(float, (u32)a[1]);
    auto b = __builtin_amdgcn_permlane32_swap(__builtin_bit_cast(u32, v), __builtin_bit_cast(u32, v), false, false);
    return __builtin_bit_cast(float, (u32)b[0]) + __builtin_bit_cast(float, (u32)b[1]);
}
DI float sum_half(float v) {
    v += __builtin_amdgcn_update_dpp(0.f, v, 0x128, 0xf, 0xf, true);
    v += __builtin_amdgcn_update_dpp(0.f, v, 0x124, 0xf, 0xf, true);
    v += __builtin_amdgcn_update_dpp(0.f, v, 0x122, 0xf, 0xf, true);
    v += __builtin_amdgcn_update_dpp(0.f, v, 0x121, 0xf, 0xf, true);
    auto a = __builtin_amdgcn_permlane16_swap(__builtin_bit_cast(u32, v), __builtin_bit_cast(u32, v), false, false);
    return __builtin_bit_cast(float, (u32)a[0]) + __builtin_bit_cast(float, (u32)a[1]);
}
DI f4 unpk(u32 a, u32 b) { return f4{blo(a), bhi(a), blo(b), bhi(b)}; }
DI v2u trd(u32 addr) { return __builtin_bit_cast(v2u, __builtin_amdgcn_ds_read_tr16_b64_v4i16((LAS s4*)addr)); }
DI f4 gelu4(f4 a, float k0, float k1) {
    const f4 t = a * (a * a * k1 + k0);
    const f4 d = f4{__builtin_amdgcn_exp2f(t[0]), __builtin_amdgcn_exp2f(t[1]), __builtin_amdgcn_exp2f(t[2]), __builtin_amdgcn_exp2f(t[3])} + 1.0f;
    return a * f4{__builtin_amdgcn_rcpf(d[0]), __builtin_amdgcn_rcpf(d[1]), __builtin_amdgcn_rcpf(d[2]), __builtin_amdgcn_rcpf(d[3])};
}
DI void sum_q2(float& a, float& b) {
    auto s = __builtin_amdgcn_permlane16_swap(__builtin_bit_cast(u32, a), __builtin_bit_cast(u32, b), false, false);
    const float v = F_(s[0]) + F_(s[1]);
    auto t = __builtin_amdgcn_permlane32_swap(__builtin_bit_cast(u32, v), __builtin_bit_cast(u32, v), false, false);
    const float u = F_(t[0]) + F_(t[1]);
    auto o = __builtin_amdgcn_permlane16_swap(__builtin_bit_cast(u32, u), __builtin_bit_cast(u32, u), false, false);
    a = F_(o[0]); b = F_(o[1]);
}
DI u32 dcol(u32 c, u32 n, u32 i) {
    const u32 pr = 64 * c + ((n >> 1) << 5) + ((i >> 2) << 3) + ((n & 1) << 2) + (i & 3), sg = 512 + 16 * c + i, m = 0u - (n >> 2);
    return (pr & ~m) | (sg & m);
}
constexpr u32 OOB = 0x40000000u;
DI void poll64(v4u sw, u32 voff, u64 tag) {
    for (int n = 0; n < SPIN_CAP; ++n) {
        u64 x;
        asm volatile("buffer_load_dwordx2 %0, %1, %2, 0 offen sc1\n s_waitcnt vmcnt(0)" : "=&v"(x) : "v"(voff), "s"(sw) : "memory");
        if (__all(x == tag)) break;
    }
}
DI void poll_mask1(v4u sw, u32 voff, u64 tag, u64& seen, u64 need) {
    for (int n = 0; (seen & need) != need && n < SPIN_CAP; ++n) {
        u64 x;
        asm volatile("buffer_load_dwordx2 %0, %1, %2, 0 offen sc1\n s_waitcnt vmcnt(0)" : "=&v"(x) : "v"(voff), "s"(sw) : "memory");
        seen = __ballot(x == tag);
    }
}
DI void poll_mask4(v4u sw, u32 voff, u64 tag, u64& seen, u64 need) {
    const u32 lo = (u32)tag, hi = (u32)(tag >> 32);
    for (int n = 0; (seen & need) != need && n < SPIN_CAP; ++n) {
        v4u x, y;
        asm volatile("buffer_load_dwordx4 %0, %2, %3, 0 offen sc1\n buffer_load_dwordx4 %1, %2, %3, 0 offen offset:16 sc1\n s_waitcnt vmcnt(0)" : "=&v"(x), "=&v"(y) : "v"(voff), "s"(sw) : "memory");
        u32 a = (x.x ^ lo) | (x.z ^ lo) | (y.x ^ lo) | (y.z ^ lo), b = (x.y ^ hi) | (x.w ^ hi) | (y.y ^ hi) | (y.w ^ hi);
        asm volatile("" : "+v"(a), "+v"(b));
        seen = __ballot((a | b) == 0);
    }
}
DI u32 spread(u32 t) { return t + (t & ~8191u); }
DI u32 ord(u32 j, u32 c) { return j == 0 ? c : j - (j <= c ? 1 : 0); }
template <int CH, int RW> DI u32 lead(u32 i, u32 o, u32 c) {
    const u32 j = i - o, t = CH == 24 ? j + 3 * c : (j & ~7u) + ((j + c) & 7);
    return i < o || j >= 24 ? (i + RW * c) & 7 : 8 + (t >= 24 ? t - 24 : t);
}
DI u32 ord3(u32 k, u32 c) { const u32 j = (k * 86) >> 8; return ord(j, c) * 3 + (k - j * 3); }
DI v4u bperm4(u32 a, v4u v) { return v4u{(u32)__builtin_amdgcn_ds_bpermute(a, v.x), (u32)__builtin_amdgcn_ds_bpermute(a, v.y), (u32)__builtin_amdgcn_ds_bpermute(a, v.z), (u32)__builtin_amdgcn_ds_bpermute(a, v.w)}; }
DI v2u bperm2(u32 a, v2u v) { return v2u{(u32)__builtin_amdgcn_ds_bpermute(a, v.x), (u32)__builtin_amdgcn_ds_bpermute(a, v.y)}; }
DI void put64(v4u sw, u32 voff, u64 tag) { asm volatile("buffer_store_dwordx2 %0, %1, %2, 0 offen sc1" :: "v"(tag), "v"(voff), "s"(sw) : "memory"); }

constexpr u32 CS_LDS = 159744, SCR_LDS = 143360, DUMMY_LDS = 160768, SIDE_LDS = 160640, XN_LDS = 43008, RT_LDS = 69632, RT2_LDS = 96256, CSP_LDS = 143360;
constexpr u32 CODE_PF_BYTES = 57344, PFL = 13; constexpr int OPT_LF = 8, OPT_RW = 0, NSTG = 14;
__global__ __launch_bounds__(512, 2) void fnet_gmlp_fused(const u16* __restrict__ x, char* ws0, const u16* __restrict__ win, const u16* __restrict__ wsr,
        const u16* __restrict__ wsi, const u16* __restrict__ wdr, const u16* __restrict__ wdi, const u16* __restrict__ wsp,
        const u16* __restrict__ bsp, const u16* __restrict__ wout, u16* __restrict__ out) {
    extern __shared__ __attribute__((aligned(16))) char smem[];
    const u32 b = blockIdx.x, lane = threadIdx.x & 63, w8 = __builtin_amdgcn_readfirstlane(threadIdx.x >> 6);
    u64 pc; asm volatile("s_getpc_b64 %0" : "=s"(pc));
    const v4u sc = srd((const void*)(pc & ~127ull), CODE_PF_BYTES);
    if (CODE_PF_BYTES) {
        const u32 vc = (threadIdx.x + (threadIdx.x < 32 ? 0 : (b >> 3) * PFL)) * 128 | (threadIdx.x < 32 + PFL ? 0 : OOB);
        asm volatile("s_mov_b32 m0, %0\n s_nop 0\n buffer_load_dword %1, %2, 0 offen lds" :: "s"(DUMMY_LDS), "v"(vc), "s"(sc) : "memory");
        SB;
    }
    asm volatile("" :: "s"(x), "s"(ws0), "s"(win), "s"(wsr), "s"(wsi), "s"(wdr), "s"(wdi), "s"(wsp), "s"(bsp), "s"(wout), "s"(out));
    const u32 grp = b & 7, p = grp * 4 + (b >> 6), c = (b >> 3) & 7;
    const u32 r = lane & 15, q = lane >> 4, ta = r >> 2, tb = lane & 3;
    const u32 vlin = lane * 16;
    const u32 rotA = grp * 4, rotH = rotA;
    char* ws = (char*)(((u64)ws0 + (2 * MB - 1)) & ~(u64)(2 * MB - 1));
    const v4u sw = srd(ws, WS_BYTES);
    const __amdgpu_buffer_rsrc_t rw = __builtin_amdgcn_make_buffer_rsrc(ws, 0, WS_BYTES, 0x00020000);
#define ASK_TAG() u64 gen_; asm volatile("s_load_dwordx2 %0, %1, 0" : "=&s"(gen_) : "s"(ws) : "memory"); SB
#define GET_TAG() SB; asm volatile("s_waitcnt lgkmcnt(0)" : "+s"(gen_) :: "memory"); SB; const u64 tag = gen_ + 0x9E3779B97F4A7C15ull

    if (w8 >= 4) {
        const u32 lw = w8 - 4;
        ASK_TAG();
        const v4u sre = srd(wsr, 2048 * 4096), sim = srd(wsi, 2048 * 4096), ssp = srd(wsp, 2048 * 4096), swd = srd(lw < 2 ? wdr : wdi, 640 * 1280);
        const u32 md = DUMMY_LDS + lw * 256;
#define TOUCH(v, s, so) asm volatile("s_mov_b32 m0, %0\n s_nop 0\n buffer_load_dword %1, %2, %3 offen lds" :: "s"(md), "v"(v), "s"(s), "s"(so) : "memory")
        const v4u sws = srd((lw & 1) ? wsi : wsr, 2048 * 4096);
        const u32 vtr = (p * 64 + c * 8 + (lane >> 3)) * 4096;
#define VT(j, n) ((vtr + ((((j) + rotA + (lane & 7)) & 31) << 7)) | ((lane & 7) < (n) ? 0 : OOB))
#define TOUCH_F0() asm volatile("s_mov_b32 m0, %0\n s_nop 0\n buffer_load_dword %1, %2, 0 offen sc1 lds" :: "s"(md), \
        "v"((F0_OFF + (b >> 5) * CELL + (b & 31) * 128) | (threadIdx.x == 384 ? 0 : OOB)), "s"(sw) : "memory")
#define TOUCH_WSA(n) TOUCH((vtr + ((lw * (n) + (lane & 7)) << 7)) | ((lane & 7) < (n) ? 0 : OOB), ssp, 0u)
        const char* pfs = (const char*)wsp + (p * 64 + c * 8 + lw * 2) * 4096 + 60;
#define SPF(base, off) asm volatile("s_load_dwordx2 s[96:97], %0, " #off :: "s"(base) : "s96", "s97")
#define TOUCH_WSH(j, n) TOUCH(VT(j, n), sws, 0u)
#define L_PROBE() do { u64 t0_, t1_; u32 d_; \
        asm volatile("s_memtime %0\n s_waitcnt lgkmcnt(0)\n s_load_dword %2, %3, %4\n s_waitcnt lgkmcnt(0)\n s_memtime %1\n s_waitcnt lgkmcnt(0)" : "=&s"(t0_), "=&s"(t1_), "=&s"(d_) \
                     : "s"(ws), "s"(PRB_OFF + (b >> 4) * CELL + (grp & 1) * 8192 + (b & 15) * 512) : "memory"); \
        const u32 l_ = ((u32)t1_ - (u32)t0_) >> 4; *(LAS u32*)SIDE_LDS = l_ < 255 ? l_ : 255; WAITL0; } while (0)
#define TOUCH_WD() TOUCH(((b * 2 + (lw & 1)) * 13 + (lane < 13 ? lane : 0)) * 128, swd, 0u)
        if (lw >= 2) { TOUCH_F0(); TOUCH_WSH(0, 4); }
        const u32 va0 = (p * 64 + lw * 16 + (lane >> 3)) * 4096 + (((lane & 7) ^ q) << 4);
        const u32 va1 = (p * 64 + lw * 16 + 8 + (lane >> 3)) * 4096 + (((lane & 7) ^ (4 + q)) << 4) - 1024;
        const u32 m4 = lw * 4096, m25 = lw * 2560, m5 = lw * 5120, m3 = lw * 3072, m2 = lw * 2048;
#define A_XT(ks) (XT_OFF + (c * 32 + (ks)) * 10240 + m25)
#define A_ZT(ks) (ZT_OFF + (c * 32 + (ks)) * 12288 + m3)
        const u32 sWI = WI_OFF + c * 240 * 1024 + m3, sWO = WO_OFF + c * 128 * 1024;
#define A_WI(part, u) (sWI + ((part) * 10 + (u)) * 12288)
#define A_WO(kb) (sWO + (kb) * 5120)
#define DM "buffer_load_dwordx4 "
#define LD_A2(off, so) asm volatile("s_add_u32 m0, %0, " #off "\n s_nop 1\n" DM "%1, %3, %5 offen lds\n" DM "%2, %3, %5 offen offset:1024 lds\n" \
        "s_add_u32 m0, m0, 2048\n s_nop 0\n" DM "%1, %4, %5 offen lds\n" DM "%2, %4, %5 offen offset:1024 lds" \
        :: "s"(m4), "v"(va0), "v"(va1), "s"(sre), "s"(sim), "s"(so) : "memory", "scc")
#define LD_A2H(off, so, sr) asm volatile("s_add_u32 m0, %0, " #off "\n s_nop 1\n" DM "%1, %3, %4 offen lds\n" DM "%2, %3, %4 offen offset:1024 lds" \
        :: "s"(m4), "v"(va0), "v"(va1), "s"(sr), "s"(so) : "memory", "scc")
        u32 vwd[5];
#pragma unroll
        for (int v = 0; v < 5; ++v) { const u32 u = (lw & 1) * 5 + v, h = u & 1; vwd[v] = dcol(c, u >> 1, h * 8 + (lane >> 3)) * 1280 + (((lane & 7) ^ (h * 4 + q)) << 4); }
        asm volatile("" : "+v"(vwd[0]), "+v"(vwd[1]), "+v"(vwd[2]), "+v"(vwd[3]), "+v"(vwd[4]));
#define LD_A4(off, so) asm volatile("s_add_u32 m0, %0, " #off "\n s_nop 1\n" DM "%1, %3, %4 offen lds\n" DM "%2, %3, %4 offen offset:1024 lds" \
        :: "s"(m2), "v"(va0), "v"(va1), "s"(ssp), "s"(so) : "memory", "scc")
#define LD_B2(off, so) asm volatile("s_add_u32 m0, %0, " #off "\n s_nop 1\n" DM "%1, %2, %3 offen sc1 lds\n" DM "%1, %2, %3 offen offset:1024 sc1 lds\n" \
        "s_mov_b32 exec_hi, 0\n" DM "%1, %2, %3 offen offset:2048 sc1 lds\n s_mov_b32 exec_hi, -1" \
        :: "s"(m25), "v"(vlin), "s"(sw), "s"(so) : "memory", "scc")
#define LD_3(off, so) asm volatile("s_add_u32 m0, %0, " #off "\n s_nop 1\n" DM "%1, %2, %3 offen sc1 lds\n" DM "%1, %2, %3 offen offset:1024 sc1 lds\n" \
        DM "%1, %2, %3 offen offset:2048 sc1 lds" :: "s"(m3), "v"(vlin), "s"(sw), "s"(so) : "memory", "scc")
#define LD_5(off, so) asm volatile("s_add_u32 m0, %0, " #off "\n s_nop 0\n" DM "%1, %2, %3 offen sc1 lds\n" DM "%1, %2, %3 offen offset:1024 sc1 lds\n" \
        DM "%1, %2, %3 offen offset:2048 sc1 lds\n" DM "%1, %2, %3 offen offset:3072 sc1 lds\n s_add_u32 m0, m0, 4096\n s_nop 0\n" DM "%1, %2, %4 offen sc1 lds" \
        :: "s"(m5), "v"(vlin), "s"(sw), "s"(so), "s"((so) + 4096u) : "memory", "scc")
#define LD_2(off, so) asm volatile("s_add_u32 m0, %0, " #off "\n s_nop 0\n" DM "%1, %2, %3 offen sc1 lds\n" DM "%1, %2, %3 offen offset:1024 sc1 lds" \
        :: "s"(m2), "v"(vlin), "s"(sw), "s"(so) : "memory", "scc")
#define LD_WD(off, so) asm volatile("s_add_u32 m0, %0, " #off "\n s_nop 0\n" DM "%1, %6, %7 offen lds\n s_add_u32 m0, m0, 1024\n s_nop 0\n" DM "%2, %6, %7 offen lds\n" \
        "s_add_u32 m0, m0, 1024\n s_nop 0\n" DM "%3, %6, %7 offen lds\n s_add_u32 m0, m0, 1024\n s_nop 0\n" DM "%4, %6, %7 offen lds\n" \
        "s_add_u32 m0, m0, 1024\n s_nop 0\n" DM "%5, %6, %7 offen lds" \
        :: "s"(m5), "v"(vwd[0]), "v"(vwd[1]), "v"(vwd[2]), "v"(vwd[3]), "v"(vwd[4]), "s"(swd), "s"(so) : "memory", "scc")
#define FLAG_F0() do { put64(sw, (F0_OFF + lane * CELL + (p * 8 + c) * 8) | (threadIdx.x < 264 ? 0 : OOB), tag); __builtin_amdgcn_s_sleep(1); } while (0)
        u32 rE0 = c, rE1 = c, rD = c, rO = c, oA2 = 2 * (p & 3) + 2;
        asm volatile("" : "+s"(rE0), "+s"(rE1), "+s"(rD), "+s"(rO), "+s"(oA2));
        u32 tabA = ((lead<OPT_LF, OPT_RW>(lane & 31, oA2, c) + rotA) & 31), tabB = A_XT(tabA); tabA *= 128;
        asm volatile("" : "+v"(tabA), "+v"(tabB));
        u32 tabC = (lane + rotH) & 31, tabD = A_ZT(tabC); tabC *= 128;
        asm volatile("" : "+v"(tabC), "+v"(tabD));
#define RDL(name, tab, i) u32 name = __builtin_amdgcn_readlane(tab, i); asm volatile("" : "+s"(name))
        u64 seen0 = 0, seen4 = 0;
        const u32 vf0 = F0_OFF + grp * CELL + ((((lane >> 1) + rotA) & 31) * 8 + (lane & 1) * 4) * 8;
        u32 vf4, wf4;
#define L_SIDE() do { const u32 k_ = *(const LAS u32*)(SIDE_LDS + 4); vf4 = GRP_OFF + grp * CELL + (((k_ ^ (grp >> 2)) & 1) << 13) + c * 256 + ((lane + rotH) & 31) * 8; \
        wf4 = (GRP_OFF + lane * CELL + (((k_ ^ (lane >> 2)) & 1) << 13) + c * 256 + p * 8) | (threadIdx.x < 264 ? 0 : OOB); asm volatile("" : "+v"(vf4), "+v"(wf4)); } while (0)
#define NEED_F0(m) poll_mask4(sw, vf0, tag, seen0, m)
#define NEED_F4(m) poll_mask1(sw, vf4, tag, seen4, m)
#define FLAG_F4() do { put64(sw, wf4, tag); __builtin_amdgcn_s_sleep(1); } while (0)
        GET_TAG();
        BAR();
            if (lw == 0) L_PROBE(); else if (lw >= 2) { __builtin_amdgcn_s_sleep(6); TOUCH_WSH(4, 4); } RDL(kB0, tabB, 0); RDL(kB1, tabB, 1); RDL(kB2, tabB, 2); RDL(kA0, tabA, 0); RDL(kA1, tabA, 1); RDL(kA2, tabA, 2); RDL(kA3, tabA, 3); asm volatile("s_nop 2");
        BAR();
        FLAG_F0();
        LD_A2(106496, kA0);
        LD_A2(133120, kA1);
        BAR();
        __builtin_amdgcn_s_sleep(4);
        NEED_F0(0xffffffffffffffffull);
        LD_B2(122880, kB0);
        LD_B2(149504, kB1);
        LD_B2(16384, kB2);
        LD_A2(0, kA2);
        LD_A2(26624, kA3);
        RDL(kB3, tabB, 3); RDL(kA4, tabA, 4); asm volatile("s_nop 2");
        WAITV(14);
        BAR();
        LD_B2(43008, kB3);
        LD_A2(53248, kA4);
        SPF(pfs, 1024); SPF(pfs, 5120);
        RDL(kB4, tabB, 4); RDL(kA5, tabA, 5); asm volatile("s_nop 2");
        WAITV(18);
        BAR();
        LD_B2(69632, kB4);
        LD_A2(79872, kA5);
        SPF(pfs, 1152); SPF(pfs, 5248);
        RDL(kB5, tabB, 5); RDL(kA6, tabA, 6); asm volatile("s_nop 2");
        WAITV(18);
        BAR();
        LD_B2(96256, kB5);
        LD_A2(106496, kA6);
        SPF(pfs, 1280); SPF(pfs, 5376);
        RDL(kB6, tabB, 6); RDL(kA7, tabA, 7); asm volatile("s_nop 2");
        WAITV(18);
        BAR();
        LD_B2(122880, kB6);
        LD_A2(133120, kA7);
        SPF(pfs, 1408); SPF(pfs, 5504);
        RDL(kB7, tabB, 7); RDL(kA8, tabA, 8); asm volatile("s_nop 2");
        WAITV(18);
        BAR();
        LD_B2(149504, kB7);
        LD_A2(0, kA8);
        if (((p + (p >> 2)) & 7) == 0) { TOUCH_WSA(2); TOUCH_WD(); }
        SPF(pfs, 1536); SPF(pfs, 5632);
        RDL(kB8, tabB, 8); RDL(kA9, tabA, 9); asm volatile("s_nop 2");
        WAITV(18);
        BAR();
        LD_B2(16384, kB8);
        LD_A2(26624, kA9);
        SPF(pfs, 1664); SPF(pfs, 5760);
        RDL(kB9, tabB, 9); RDL(kA10, tabA, 10); asm volatile("s_nop 2");
        WAITV(18);
        BAR();
        LD_B2(43008, kB9);
        LD_A2(53248, kA10);
        if (((p + (p >> 2)) & 7) == 1) { TOUCH_WSA(2); TOUCH_WD(); }
        SPF(pfs, 1792); SPF(pfs, 5888);
        RDL(kB10, tabB, 10); RDL(kA11, tabA, 11); asm volatile("s_nop 2");
        WAITV(18);
        BAR();
        LD_B2(69632, kB10);
        LD_A2(79872, kA11);
        SPF(pfs, 1920); SPF(pfs, 6016);
        RDL(kB11, tabB, 11); RDL(kA12, tabA, 12); asm volatile("s_nop 2");
        WAITV(18);
        BAR();
        LD_B2(96256, kB11);
        LD_A2(106496, kA12);
        if (((p + (p >> 2)) & 7) == 2) { TOUCH_WSA(2); TOUCH_WD(); }
        SPF(pfs, 2048); SPF(pfs, 6144);
        RDL(kB12, tabB, 12); RDL(kA13, tabA, 13); asm volatile("s_nop 2");
        WAITV(18);
        BAR();
        LD_B2(122880, kB12);
        LD_A2(133120, kA13);
        SPF(pfs, 2176); SPF(pfs, 6272);
        RDL(kB13, tabB, 13); RDL(kA14, tabA, 14); asm volatile("s_nop 2");
        WAITV(18);
        BAR();
        LD_B2(149504, kB13);
        LD_A2(0, kA14);
        if (((p + (p >> 2)) & 7) == 3) { TOUCH_WSA(2); TOUCH_WD(); }
        SPF(pfs, 2304); SPF(pfs, 6400);
        RDL(kB14, tabB, 14); RDL(kA15, tabA, 15); asm volatile("s_nop 2");
        WAITV(18);
        BAR();
        LD_B2(16384, kB14);
        LD_A2(26624, kA15);
        SPF(pfs, 2432); SPF(pfs, 6528);
        RDL(kB15, tabB, 15); RDL(kA16, tabA, 16); asm volatile("s_nop 2");
        WAITV(18);
        BAR();
        LD_B2(43008, kB15);
        LD_A2(53248, kA16);
        if (((p + (p >> 2)) & 7) == 4) { TOUCH_WSA(2); TOUCH_WD(); }
        SPF(pfs, 2560); SPF(pfs, 6656);
        RDL(kB16, tabB, 16); RDL(kA17, tabA, 17); asm volatile("s_nop 2");
        WAITV(18);
        BAR();
        LD_B2(69632, kB16);
        LD_A2(79872, kA17);
        SPF(pfs, 2688); SPF(pfs, 6784);
        RDL(kB17, tabB, 17); RDL(kA18, tabA, 18); asm volatile("s_nop 2");
        WAITV(18);
        BAR();
        LD_B2(96256, kB17);
        LD_A2(106496, kA18);
        if (((p + (p >> 2)) & 7) == 5) { TOUCH_WSA(2); TOUCH_WD(); }
        SPF(pfs, 2816); SPF(pfs, 6912);
        RDL(kB18, tabB, 18); RDL(kA19, tabA, 19); asm volatile("s_nop 2");
        WAITV(18);
        BAR();
        LD_B2(122880, kB18);
        LD_A2(133120, kA19);
        SPF(pfs, 2944); SPF(pfs, 7040);
        RDL(kB19, tabB, 19); RDL(kA20, tabA, 20); asm volatile("s_nop 2");
        WAITV(18);
        BAR();
        LD_B2(149504, kB19);
        LD_A2(0, kA20);
        if (((p + (p >> 2)) & 7) == 6) { TOUCH_WSA(2); TOUCH_WD(); }
        SPF(pfs, 3072); SPF(pfs, 7168);
        RDL(kB20, tabB, 20); RDL(kA21, tabA, 21); asm volatile("s_nop 2");
        WAITV(18);
        BAR();
        LD_B2(16384, kB20);
        LD_A2(26624, kA21);
        SPF(pfs, 3200); SPF(pfs, 7296);
        RDL(kB21, tabB, 21); RDL(kA22, tabA, 22); asm volatile("s_nop 2");
        WAITV(18);
        BAR();
        LD_B2(43008, kB21);
        LD_A2(53248, kA22);
        if (((p + (p >> 2)) & 7) == 7) { TOUCH_WSA(2); TOUCH_WD(); }
        SPF(pfs, 3328); SPF(pfs, 7424);
        RDL(kB22, tabB, 22); RDL(kA23, tabA, 23); asm volatile("s_nop 2");
        WAITV(18);
        BAR();
        LD_B2(69632, kB22);
        LD_A2(79872, kA23);
        SPF(pfs, 3456); SPF(pfs, 7552);
        RDL(kB23, tabB, 23); RDL(kA24, tabA, 24); asm volatile("s_nop 2");
        WAITV(18);
        BAR();
        LD_B2(96256, kB23);
        LD_A2(106496, kA24);
        SPF(pfs, 3584); SPF(pfs, 7680);
        RDL(kB24, tabB, 24); RDL(kA25, tabA, 25); asm volatile("s_nop 2");
        WAITV(18);
        BAR();
        LD_B2(122880, kB24);
        LD_A2(133120, kA25);
        SPF(pfs, 3712); SPF(pfs, 7808);
        RDL(kB25, tabB, 25); RDL(kA26, tabA, 26); asm volatile("s_nop 2");
        WAITV(18);
        BAR();
        LD_B2(149504, kB25);
        LD_A2(0, kA26);
        SPF(pfs, 3840); SPF(pfs, 7936);
        RDL(kB26, tabB, 26); RDL(kA27, tabA, 27); asm volatile("s_nop 2");
        WAITV(18);
        BAR();
        LD_B2(16384, kB26);
        LD_A2(26624, kA27);
        SPF(pfs, 3968); SPF(pfs, 8064);
        RDL(kB27, tabB, 27); RDL(kA28, tabA, 28); asm volatile("s_nop 2");
        WAITV(18);
        BAR();
        LD_B2(43008, kB27);
        LD_A2(53248, kA28);
        RDL(kB28, tabB, 28); RDL(kA29, tabA, 29); asm volatile("s_nop 2");
        WAITV(18);
        BAR();
        LD_B2(69632, kB28);
        LD_A2(79872, kA29);
        RDL(kB29, tabB, 29); RDL(kA30, tabA, 30); asm volatile("s_nop 2");
        WAITV(18);
        BAR();
        LD_B2(96256, kB29);
        LD_A2(106496, kA30);
        RDL(kB30, tabB, 30); RDL(kA31, tabA, 31); asm volatile("s_nop 2");
        WAITV(18);
        BAR();
        LD_B2(122880, kB30);
        LD_A2(133120, kA31);
        RDL(kB31, tabB, 31); asm volatile("s_nop 2");
        WAITV(18);
        BAR();
        LD_B2(149504, kB31);
        LD_WD(0, ord(0, rD) * 128u);
        WAITV(19);
        BAR();
        LD_WD(20480, ord(1, rD) * 128u);
        WAITV(17);
        BAR();
        LD_WD(40960, ord(2, rD) * 128u);
        WAITV(15);
        BAR();
        WAITV(10);
        BAR();
        LD_WD(61440, ord(3, rD) * 128u);
        WAITV(10);
        BAR();
        LD_WD(81920, ord(4, rD) * 128u);
        LD_WD(102400, ord(5, rD) * 128u);
        LD_WD(122880, ord(6, rD) * 128u);
        WAITV(20);
        BAR();
        LD_WD(0, ord(7, rD) * 128u);
        WAITV(20);
        BAR();
        LD_WD(20480, ord(8, rD) * 128u);
        WAITV(20);
        BAR();
        LD_WD(40960, ord(9, rD) * 128u);
        WAITV(20);
        BAR();
        WAITV(15);
        BAR();
        LD_2(143360, A_WO(24) + m2);
        WAITV(12);
        BAR();
        LD_3(61440, A_WI(0, ord(0, rE0)));
        LD_3(73728, A_WI(0, ord(1, rE0)));
        WAITV(13);
        BAR();
        LD_3(86016, A_WI(0, ord(2, rE0)));
        LD_3(98304, A_WI(0, ord(3, rE0)));
        WAITV(14);
        BAR();
        WAITV(9);
        BAR();
        LD_3(110592, A_WI(0, ord(4, rE0)));
        LD_3(122880, A_WI(0, ord(5, rE0)));
        WAITV(9);
        BAR();
        LD_3(0, A_WI(0, ord(6, rE0)));
        LD_3(12288, A_WI(0, ord(7, rE0)));
        LD_3(24576, A_WI(0, ord(8, rE0)));
        LD_3(36864, A_WI(0, ord(9, rE0)));
        WAITV(15);
        BAR();
        WAITV(9);
        BAR();
        LD_3(49152, A_WI(1, ord(0, rE1)));
        LD_3(61440, A_WI(1, ord(1, rE1)));
        WAITV(9);
        BAR();
        LD_3(73728, A_WI(1, ord(2, rE1)));
        LD_3(86016, A_WI(1, ord(3, rE1)));
        WAITV(12);
        BAR();
        LD_3(98304, A_WI(1, ord(4, rE1)));
        LD_3(110592, A_WI(1, ord(5, rE1)));
        RDL(kC20, tabC, 20); RDL(kC25, tabC, 25); RDL(kC29, tabC, 29); asm volatile("s_nop 2");
        BAR();
        LD_A4(0, kC20);
        LD_A4(8192, kC25);
        LD_A4(16384, kC29);
        LD_3(122880, A_WI(1, ord(6, rE1)));
        LD_3(24576, A_WI(1, ord(9, rE1)));
        L_SIDE();
        RDL(kC2, tabC, 2); RDL(kC6, tabC, 6); asm volatile("s_nop 2");
        WAITV(6);
        BAR();
        LD_A2H(143360, kC2, ssp); LD_A2H(145408, kC6, ssp);
        RDL(kC11, tabC, 11); RDL(kC16, tabC, 16); asm volatile("s_nop 2");
        WAITV(0);
        BAR();
        LD_A4(36864, kC11);
        LD_A4(135168, kC16);
        LD_3(0, A_WI(1, ord(7, rE1)));
        LD_3(12288, A_WI(1, ord(8, rE1)));
        RDL(kC0, tabC, 0); RDL(kC1, tabC, 1); RDL(kC3, tabC, 3); asm volatile("s_nop 2");
        WAITV(6);
        BAR();
        LD_A4(61440, kC0);
        LD_A4(69632, kC1);
        LD_A4(77824, kC3);
        RDL(kD0, tabD, 0); RDL(kD1, tabD, 1); RDL(kD2, tabD, 2); RDL(kC4, tabC, 4); RDL(kC5, tabC, 5); RDL(kC7, tabC, 7); RDL(kC8, tabC, 8); RDL(kC9, tabC, 9); RDL(kC10, tabC, 10); RDL(kC12, tabC, 12); asm volatile("s_nop 2");
        WAITV(6);
        BAR();
        FLAG_F4();
        LD_A4(86016, kC4);
        LD_A4(94208, kC5);
        LD_A4(102400, kC7);
        LD_A4(110592, kC8);
        LD_A4(118784, kC9);
        LD_A4(126976, kC10);
        LD_A4(135168, kC12);
        __builtin_amdgcn_s_sleep(4);
        NEED_F4(0xffffffffull);
        LD_3(36864, kD0);
        LD_3(147456, kD1);
        LD_3(49152, kD2);
        RDL(kD3, tabD, 3); RDL(kD4, tabD, 4); asm volatile("s_nop 2");
        WAITV(6);
        BAR();
        LD_3(0, kD3);
        LD_3(12288, kD4);
        RDL(kD5, tabD, 5); asm volatile("s_nop 2");
        WAITV(9);
        BAR();
        LD_3(24576, kD5);
        RDL(kD6, tabD, 6); RDL(kC13, tabC, 13); asm volatile("s_nop 2");
        WAITV(9);
        BAR();
        LD_3(36864, kD6);
        LD_A4(61440, kC13);
        RDL(kD7, tabD, 7); RDL(kC14, tabC, 14); asm volatile("s_nop 2");
        WAITV(11);
        BAR();
        LD_3(147456, kD7);
        LD_A4(69632, kC14);
        RDL(kD8, tabD, 8); asm volatile("s_nop 2");
        WAITV(13);
        BAR();
        LD_3(49152, kD8);
        RDL(kD9, tabD, 9); RDL(kC15, tabC, 15); asm volatile("s_nop 2");
        WAITV(13);
        BAR();
        LD_3(0, kD9);
        LD_A4(77824, kC15);
        RDL(kD10, tabD, 10); RDL(kC17, tabC, 17); asm volatile("s_nop 2");
        WAITV(15);
        BAR();
        LD_3(12288, kD10);
        LD_A4(86016, kC17);
        RDL(kD11, tabD, 11); asm volatile("s_nop 2");
        WAITV(15);
        BAR();
        LD_3(24576, kD11);
        RDL(kD12, tabD, 12); RDL(kC18, tabC, 18); asm volatile("s_nop 2");
        WAITV(13);
        BAR();
        LD_3(36864, kD12);
        LD_A4(94208, kC18);
        RDL(kD13, tabD, 13); asm volatile("s_nop 2");
        WAITV(15);
        BAR();
        LD_3(147456, kD13);
        RDL(kD14, tabD, 14); RDL(kC19, tabC, 19); asm volatile("s_nop 2");
        WAITV(13);
        BAR();
        LD_3(49152, kD14);
        LD_A4(102400, kC19);
        RDL(kD15, tabD, 15); RDL(kC21, tabC, 21); asm volatile("s_nop 2");
        WAITV(13);
        BAR();
        LD_3(0, kD15);
        LD_A4(110592, kC21);
        RDL(kD16, tabD, 16); asm volatile("s_nop 2");
        WAITV(15);
        BAR();
        LD_3(12288, kD16);
        RDL(kD17, tabD, 17); RDL(kC22, tabC, 22); asm volatile("s_nop 2");
        WAITV(13);
        BAR();
        LD_3(24576, kD17);
        LD_A4(118784, kC22);
        RDL(kD18, tabD, 18); asm volatile("s_nop 2");
        WAITV(15);
        BAR();
        LD_3(36864, kD18);
        RDL(kD19, tabD, 19); RDL(kC23, tabC, 23); asm volatile("s_nop 2");
        WAITV(13);
        BAR();
        LD_3(147456, kD19);
        LD_A4(126976, kC23);
        RDL(kD20, tabD, 20); RDL(kC24, tabC, 24); asm volatile("s_nop 2");
        WAITV(13);
        BAR();
        LD_3(49152, kD20);
        LD_A4(135168, kC24);
        RDL(kD21, tabD, 21); asm volatile("s_nop 2");
        WAITV(15);
        BAR();
        LD_3(0, kD21);
        RDL(kD22, tabD, 22); RDL(kC26, tabC, 26); asm volatile("s_nop 2");
        WAITV(13);
        BAR();
        LD_3(12288, kD22);
        LD_A4(61440, kC26);
        RDL(kD23, tabD, 23); asm volatile("s_nop 2");
        WAITV(15);
        BAR();
        LD_3(24576, kD23);
        RDL(kD24, tabD, 24); RDL(kC27, tabC, 27); asm volatile("s_nop 2");
        WAITV(13);
        BAR();
        LD_3(36864, kD24);
        LD_A4(69632, kC27);
        RDL(kD25, tabD, 25); asm volatile("s_nop 2");
        WAITV(13);
        BAR();
        LD_3(147456, kD25);
        RDL(kD26, tabD, 26); RDL(kC28, tabC, 28); asm volatile("s_nop 2");
        WAITV(13);
        BAR();
        LD_3(49152, kD26);
        LD_A4(77824, kC28);
        RDL(kD27, tabD, 27); RDL(kC30, tabC, 30); asm volatile("s_nop 2");
        WAITV(13);
        BAR();
        LD_3(0, kD27);
        LD_A4(86016, kC30);
        RDL(kD28, tabD, 28); asm volatile("s_nop 2");
        WAITV(15);
        BAR();
        LD_3(12288, kD28);
        RDL(kD29, tabD, 29); RDL(kC31, tabC, 31); asm volatile("s_nop 2");
        WAITV(13);
        BAR();
        LD_3(24576, kD29);
        LD_A4(94208, kC31);
        RDL(kD30, tabD, 30); asm volatile("s_nop 2");
        WAITV(15);
        BAR();
        LD_3(36864, kD30);
        RDL(kD31, tabD, 31); asm volatile("s_nop 2");
        WAITV(13);
        BAR();
        LD_3(147456, kD31);
        WAITV(11);
        BAR();
        LD_5(102400, A_WO(ord3(0 + lw, rO)));
        WAITV(13);
        BAR();
        LD_5(122880, A_WO(ord3(4 + lw, rO)));
        WAITV(13);
        BAR();
        LD_5(49152, A_WO(ord3(8 + lw, rO)));
        WAITV(15);
        BAR();
        WAITV(10);
        BAR();
        LD_5(0, A_WO(ord3(12 + lw, rO)));
        WAITV(5);
        BAR();
        LD_5(20480, A_WO(ord3(16 + lw, rO)));
        LD_5(69632, A_WO(ord3(20 + lw, rO)));
        WAITV(0);
        BAR();
        asm volatile("s_waitcnt lgkmcnt(0)" ::: "memory");
    } else {
        const u32 w = w8, pw = p * 4 + w, odd = c & 1;
        const __amdgpu_buffer_rsrc_t rx = __builtin_amdgcn_make_buffer_rsrc((void*)x, 0, 2048 * 1280, 0x00020000);
        v4u wo, wi0, wi1; u32 wod, wid, csd; float csv; const u32 bu = b < 240 ? b : 0, okb = bu / 10, oL = bu - okb * 10, ik2 = bu / 24, iL = bu - ik2 * 24, rto = b < 240 ? 0 : OOB;
#define C_RT_LD() do { const __amdgpu_buffer_rsrc_t ro = __builtin_amdgcn_make_buffer_rsrc((void*)wout, 0, 768 * 1280, 0x00020000), \
            ri = __builtin_amdgcn_make_buffer_rsrc((void*)win, 0, 640 * 3072, 0x00020000); \
        wi0 = __builtin_amdgcn_raw_buffer_load_b128(ri, ((lane >> 3) * 3072 + (lane & 7) * 16) | rto, (ik2 * 64 + w * 16) * 3072 + iL * 128, 2); \
        wi1 = __builtin_amdgcn_raw_buffer_load_b128(ri, ((lane >> 3) * 3072 + (lane & 7) * 16) | rto, (ik2 * 64 + w * 16 + 8) * 3072 + iL * 128, 2); \
        wo = __builtin_amdgcn_raw_buffer_load_b128(ro, ((lane >> 3) * 1280 + (lane & 7) * 16) | rto, (okb * 32 + w * 8) * 1280 + oL * 128, 2); SB; } while (0)
#define C_RT_WR() do { *(LAS v4u*)(RT_LDS + w * 2048 + vlin) = wi0; *(LAS v4u*)(RT_LDS + w * 2048 + 1024 + vlin) = wi1; *(LAS v4u*)(RT2_LDS + w * 1024 + vlin) = wo; WAITL0; SB; } while (0)
        const u32 xo = (lane >> 5) * 1280 + (lane & 31) * 16, xo2 = (lane >> 5) * 1280 + 1024 + (lane & 31) * 8, xrow = (p * 64 + c * 8 + w * 2) * 1280;
        const v4u xa = __builtin_amdgcn_raw_buffer_load_b128(rx, xo, xrow, 0), xb = __builtin_amdgcn_raw_buffer_load_b128(rx, xo + 512, xrow, 0);
        const v2u xc = __builtin_amdgcn_raw_buffer_load_b64(rx, xo2, xrow, 0);
        SB; ASK_TAG();
        u32 xad[3], xds[3];
#pragma unroll
        for (int k = 0; k < 3; ++k) { const u32 u = w + 4 * k, T = u * 4 + q, cc = (T * 205) >> 10, n = T - cc * 5;
            xad[k] = XN_LDS + ta * 1280 + dcol(cc & 7, n, tb * 4) * 2;
            xds[k] = (XT_OFF + (p * 2 + (c >> 2)) * 5120 + (c & 3) * 256 + (cc * 64 * 5 + n) * 1024 + r * 16) | (u < 10 ? 0 : OOB); }
        asm volatile("" : "+v"(xad[0]), "+v"(xad[1]), "+v"(xad[2]), "+v"(xds[0]), "+v"(xds[1]), "+v"(xds[2]));
        GET_TAG();
        {
            f4 v[5] = {unpk(xa.x, xa.y), unpk(xa.z, xa.w), unpk(xb.x, xb.y), unpk(xb.z, xb.w), unpk(xc.x, xc.y)};
            f4 s = (v[0] + v[1]) + (v[2] + v[3]) + v[4];
            const float m = sum_half((s[0] + s[1]) + (s[2] + s[3])) * (1.0f / 640.0f);
            s = f4{0.f, 0.f, 0.f, 0.f};
#pragma unroll
            for (int i = 0; i < 5; ++i) { v[i] -= m; s += v[i] * v[i]; }
            const float rs = __builtin_amdgcn_rsqf(sum_half((s[0] + s[1]) + (s[2] + s[3])) * (1.0f / 640.0f) + LN_EPS);
#pragma unroll
            for (int i = 0; i < 5; ++i) v[i] *= rs;
            *(LAS v4u*)(XN_LDS + w * 2560 + xo) = v4u{pk(v[0][0], v[0][1]), pk(v[0][2], v[0][3]), pk(v[1][0], v[1][1]), pk(v[1][2], v[1][3])};
            *(LAS v4u*)(XN_LDS + w * 2560 + xo + 512) = v4u{pk(v[2][0], v[2][1]), pk(v[2][2], v[2][3]), pk(v[3][0], v[3][1]), pk(v[3][2], v[3][3])};
            *(LAS v2u*)(XN_LDS + w * 2560 + xo2) = v2u{pk(v[4][0], v[4][1]), pk(v[4][2], v[4][3])};
        }
        WAITL0; BAR();
        {
            v2u lo[3], hi[3];
#pragma unroll
            for (int k = 0; k < 3; ++k) { lo[k] = trd(xad[k]); hi[k] = trd(xad[k] + 4 * 1280); }
#pragma unroll
            for (int k = 0; k < 3; ++k) STG((v4u{lo[k].x, lo[k].y, hi[k].x, hi[k].y}), xds[k], 0);
        }
        u32 lin[3] = {vlin, vlin + 65536, vlin + 131072}, swz0[3], swz1[3];
#define NEW_LIN() asm volatile("" : "+v"(lin[0]), "+v"(lin[1]), "+v"(lin[2]))
#define NEW_SWZ() asm volatile("" : "+v"(swz0[0]), "+v"(swz0[1]), "+v"(swz0[2]), "+v"(swz1[0]), "+v"(swz1[1]), "+v"(swz1[2]))
#define SET_SWZ(base) do { const u32 s0_ = (base) + r * 128 + ((q ^ (r >> 1)) << 4), s1_ = (base) + r * 128 + (((4 + q) ^ (r >> 1)) << 4); \
        swz0[0] = s0_; swz0[1] = s0_ + 65536; swz0[2] = s0_ + 131072; swz1[0] = s1_; swz1[1] = s1_ + 65536; swz1[2] = s1_ + 131072; \
        asm volatile("" : "+v"(swz0[0]), "+v"(swz0[1]), "+v"(swz0[2]), "+v"(swz1[0]), "+v"(swz1[1]), "+v"(swz1[2])); } while (0)
        u32 sPQ, sH, sU;
        const u32 kb0 = 2 * c, kbs = 16 + (c >> 1);
        u32 cD = c, cE = c, cO = c; asm volatile("" : "+s"(cD), "+s"(cE), "+s"(cO));
        DRAIN(); BAR();
        v4u xr0, xr1; v2u xr2; float bias;
#define FLAG_PW(F) put64(sw, (sPQ + (F) + c * 8) | (lane ? OOB : 0), tag)
#define POLL_PW(F) poll64(sw, sPQ + (F) + (lane & 7) * 8, tag)
        const u32 vrowq = (p * 64 + w * 16 + (lane >> 2)) * 1280, cblkq = kb0 * 64 + tb * 16, csglq = kbs * 64 + odd * 32 + tb * 8, pin = (r * 4 + q) * 4, pout = (tb * 16 + (lane >> 2)) * 4;
        u32 bb;
#define C_X_LD() do { xr0 = __builtin_amdgcn_raw_buffer_load_b128(rx, vrowq + cblkq, 0, 0); xr1 = __builtin_amdgcn_raw_buffer_load_b128(rx, vrowq + cblkq + 64, 0, 0); \
        xr2 = __builtin_amdgcn_raw_buffer_load_b64(rx, vrowq + csglq, 0, 0); bb = bsp[p * 64 + w * 16 + r]; SB; } while (0)
#define C_X_FIX() do { xr0 = bperm4(pin, xr0); xr1 = bperm4(pin, xr1); xr2 = bperm2(pin, xr2); bias = blo(bb); \
        asm volatile("" : "+v"(xr0), "+v"(xr1), "+v"(xr2), "+v"(bias)); WAITL0; SB; } while (0)
#define C_RT_RD() do { const u32 db = oL * 2 + (w >> 1), k1 = w & 1; const bool sh = db >= 16; \
        const u32 col = (w >> 1) * 32 + (sh ? k1 * 16 + tb * 4 : tb * 8 + k1 * 4), ad = RT2_LDS + (q * 4 + ta) * 128 + col * 2, ai = RT_LDS + (q * 8 + ta) * 128 + w * 32 + tb * 8; \
        const v2u lo = trd(ad), hi = trd(ad + 16 * 128), a0 = trd(ai), a1 = trd(ai + 512), a2 = trd(ai + 4096), a3 = trd(ai + 4608); \
        wo = v4u{lo.x, lo.y, hi.x, hi.y}; wi0 = v4u{a0.x, a0.y, a1.x, a1.y}; wi1 = v4u{a2.x, a2.y, a3.x, a3.y}; \
        const u32 cc = sh ? 2 * (db - 16) + k1 : oL, n = sh ? 4 : w; \
        wod = (vlin + WO_OFF + cc * 128 * 1024 + okb * 5120 + n * 1024) | rto; \
        const u32 T = iL * 4 + w, T2 = T < 48 ? T : T - 48, ci = T2 / 6, ni = T2 - ci * 6, part = T < 48 ? 1 : 0; \
        wid = (vlin + WI_OFF + ((ci * 2 + part) * 10 + ik2) * 12288 + ni * 1024) | rto; \
        csd = (WO_OFF + ci * 128 * 1024 + 24 * 5120 + ik2 * 768 + part * 384 + ni * 64 + r * 4) | rto | (q ? OOB : 0); \
        asm volatile("" : "+v"(wo), "+v"(wi0), "+v"(wi1), "+v"(wod), "+v"(wid), "+v"(csd)); WAITL0; SB; } while (0)
#define C_RT_CS() do { const f4 s_ = (unpk(wi0.x, wi0.y) + unpk(wi0.z, wi0.w)) + (unpk(wi1.x, wi1.y) + unpk(wi1.z, wi1.w)); \
        csv = sum_q((s_[0] + s_[1]) + (s_[2] + s_[3])); asm volatile("" : "+v"(csv)); SB; } while (0)
#define C_RT_ST(k) do { if (k == 0) STG(wi0, wid, 0); if (k == 1) STG(wi1, wid, 6144); if (k == 2) STG(wo, wod, 0); \
        if (k == 3) __builtin_amdgcn_raw_buffer_store_b32(__builtin_bit_cast(u32, csv), rw, csd, 0, 16); SB; } while (0)
#define C_FLAG_FB() do { put64(sw, (FB_OFF + lane * CELL + (p * 8 + c) * 8) | (threadIdx.x < 8 ? 0 : OOB), tag ^ (*(const LAS u32*)SIDE_LDS & 255)); SB; } while (0)
        v4u fbx, fby; u32 fw; const u32 vfb = FB_OFF + grp * CELL + lane * 32;
#define C_ASK_FB() do { fbx = LDG(vfb, 0); fby = LDG(vfb + 16, 0); SB; } while (0)
#define FB_BAD() ({ u32 x_ = ((fbx.x ^ lo_) | (fbx.z ^ lo_) | (fby.x ^ lo_) | (fby.z ^ lo_)) >> 8, y_ = (fbx.y ^ hi_) | (fbx.w ^ hi_) | (fby.y ^ hi_) | (fby.w ^ hi_); \
        asm volatile("" : "+v"(x_), "+v"(y_)); __ballot((x_ | y_) != 0) != 0; })
#define C_CHK_FB() do { const u32 lo_ = (u32)tag, hi_ = (u32)(tag >> 32); \
        if (__builtin_expect(FB_BAD(), 0)) { int n_ = SPIN_CAP; _Pragma("unroll 1") do { \
            asm volatile("buffer_load_dwordx4 %0, %2, %3, 0 offen sc1\n buffer_load_dwordx4 %1, %2, %3, 0 offen offset:16 sc1\n s_waitcnt vmcnt(0)" : "=&v"(fbx), "=&v"(fby) : "v"(vfb), "s"(sw) : "memory"); } while (FB_BAD() && --n_); } \
        const float v_ = (float)((((fbx.x ^ lo_) + (fbx.z ^ lo_)) + ((fby.x ^ lo_) + (fby.z ^ lo_))) & 1023), d_ = sum_half((lane & 8) ? -v_ : v_); \
        const float dd_ = F_(__builtin_amdgcn_readlane(__builtin_bit_cast(u32, d_), 0)) - F_(__builtin_amdgcn_readlane(__builtin_bit_cast(u32, d_), 32)); \
        const u32 k_ = __builtin_amdgcn_readfirstlane(dd_ > 0.f ? 1 : 0), near_ = k_ ^ (grp >> 2); \
        *(LAS u32*)(SIDE_LDS + 4) = k_; sPQ = LOC_OFF + pw * 16 * CELL + (((near_ ^ (pw >> 3)) & 1) << 13); sH = sPQ + H_IN; sU = sPQ + U_IN; \
        fw = __builtin_amdgcn_raw_buffer_load_b32(rw, q < 3 ? sPQ + F1_IN + q * 128 : (lane & 2 ? sPQ + G3_IN : GRP_OFF + grp * CELL + (near_ << 13)) + c * 256 + (lane & 1) * 128, 0, 16); \
        NEW_LIN(); NEW_SWZ(); SB; } while (0)
#define C_GEN() put64(sw, b == 0 && threadIdx.x == 0 ? 0 : OOB, tag)
#define C_COLSUM() do { const u32 a_ = CSP_LDS + (w * 48 + (lane < 48 ? lane : 0)) * 4; float s_[4] = {0.f, 0.f, 0.f, 0.f}; \
        _Pragma("unroll") for (int k = 0; k < 10; ++k) s_[k & 3] += *(const LAS float*)(a_ + k * 768); \
        if (lane < 48) *(LAS float*)(CS_LDS + (w * 48 + lane) * 4) = (s_[0] + s_[1]) + (s_[2] + s_[3]); WAITL0; SB; } while (0)
#define PIN5(a) asm volatile("" : "+v"(a[0]), "+v"(a[1]), "+v"(a[2]), "+v"(a[3]), "+v"(a[4])); SB
#define PIN6(a) asm volatile("" : "+v"(a[0]), "+v"(a[1]), "+v"(a[2]), "+v"(a[3]), "+v"(a[4]), "+v"(a[5])); SB
#define INIT5(a, v0, v1, v2) do { a[0] = unpk(v0.x, v0.y); a[1] = unpk(v0.z, v0.w); a[2] = unpk(v1.x, v1.y); a[3] = unpk(v1.z, v1.w); a[4] = unpk(v2.x, v2.y); PIN5(a); } while (0)
#define PK4(a, b) (v4u{pk((a)[0], (a)[1]), pk((a)[2], (a)[3]), pk((b)[0], (b)[1]), pk((b)[2], (b)[3])})
        u32 e0, e1, e2, e3;
#define PIN_E() asm volatile("" : "+v"(e0), "+v"(e1), "+v"(e2), "+v"(e3)); SB
#define PRE_A() do { e0 = vlin + sPQ + spread(kb0 * 2048); e1 = (q & 1) * 1024 + ((q >> 1) * 16 + r) * 16 + sPQ + spread(kbs * 2048) + odd * 512; e2 = e3 = 0; asm volatile("" :: "v"(fw)); PIN_E(); } while (0)
#define PRE_D() do { e0 = vlin + sH + spread(kb0 * 1024); e1 = (((q >> 1) * 16 + r) * 16 + sH + spread(kbs * 1024) + odd * 512) | (q & 1 ? OOB : 0); \
        e2 = (r * 8 + sPQ + ST2_IN + c * 128) | (q ? OOB : 0); e3 = 0; PIN_E(); } while (0)
#define PRE_E() do { e0 = (r * 16 + sPQ + G3_IN + c * 256) | (q ? OOB : 0); e1 = SCR_LDS + w * 4096 + r * 200 + q * 8; \
        e2 = SCR_LDS + w * 4096 + ((q & 1) * 8 + ta) * 200 + (q >> 1) * 32 + tb * 8; \
        e3 = (q >> 1) * 1024 + (q & 1) * 256 + r * 16 + ZT_OFF + (c * 64 + p * 2 + (w >> 1)) * 6144 + (w & 1) * 512; PIN_E(); } while (0)
#define PRE_H() do { e0 = vlin + sU + spread(c * 4096); e1 = e2 = e3 = 0; PIN_E(); } while (0)
#define PRE_O() do { e0 = vrowq + cblkq; e1 = vrowq + csglq; e2 = pout; e3 = 0; PIN_E(); } while (0)
#define C_EPI_A() do { \
        pq[0] = PK4(accA[0][0], accA[0][1]); STG(pq[0], e0, 0); pq[1] = PK4(accA[1][0], accA[1][1]) ^ 0x80008000u; STG(pq[1], e0, 1024); \
        pq[2] = PK4(accA[0][2], accA[0][3]); STG(pq[2], e0, 2048); pq[3] = PK4(accA[1][2], accA[1][3]) ^ 0x80008000u; STG(pq[3], e0, 3072); \
        const v4u s_ = PK4(accA[0][4], accA[1][4]) ^ v4u{0u, 0u, 0x80008000u, 0x80008000u}; \
        auto t0_ = __builtin_amdgcn_permlane16_swap(s_.x, s_.z, false, false); auto t1_ = __builtin_amdgcn_permlane16_swap(s_.y, s_.w, false, false); \
        STG((v4u{t0_[0], t1_[0], t0_[1], t1_[1]}), e1, 0); } while (0)
        v4u hk0, hk1; v2u hk2;
#define STATS(acc, n_, s_, q_) f4 sv_ = acc[0], qv_ = acc[0] * acc[0]; _Pragma("unroll") for (int n = 1; n < n_; ++n) { sv_ += acc[n]; qv_ += acc[n] * acc[n]; } \
        float s_ = (sv_[0] + sv_[1]) + (sv_[2] + sv_[3]), q_ = (qv_[0] + qv_[1]) + (qv_[2] + qv_[3]); sum_q2(s_, q_)
#define C_EPI_D() do { \
        hk0 = PK4(accD[0], accD[1]); STG(hk0, e0, 0); hk1 = PK4(accD[2], accD[3]); STG(hk1, e0, 1024); \
        hk2 = v2u{pk(accD[4][0], accD[4][1]), pk(accD[4][2], accD[4][3])}; \
        auto t0_ = __builtin_amdgcn_permlane16_swap(hk2.x, hk2.x, false, false); auto t1_ = __builtin_amdgcn_permlane16_swap(hk2.y, hk2.y, false, false); \
        STG((v4u{t0_[0], t1_[0], t0_[1], t1_[1]}), e1, 0); SB; \
        STATS(accD, 5, s1_, s2_); \
        __builtin_amdgcn_raw_buffer_store_b64(v2u{__builtin_bit_cast(u32, s1_), __builtin_bit_cast(u32, s2_)}, rw, e2, 0, 16); } while (0)
        v2u st0, st1; float mu2, rs2, k0, k1, var2;
#define C_LOAD_ST2() do { st0 = __builtin_amdgcn_raw_buffer_load_b64(rw, q * 256 + r * 8, sPQ + ST2_IN, 16); \
        st1 = __builtin_amdgcn_raw_buffer_load_b64(rw, q * 256 + 128 + r * 8, sPQ + ST2_IN, 16); SB; } while (0)
#define C_RD_CS(acc, part) do { _Pragma("unroll") for (int n = 0; n < 6; ++n) acc[n] = *(const LAS f4*)(CS_LDS + (part) * 384 + n * 64 + q * 16); SB; } while (0)
#define C_ST2() do { mu2 = F_(st0.x) + F_(st1.x); var2 = F_(st0.y) + F_(st1.y); sum_q2(mu2, var2); mu2 *= -1.0f / 640.0f; SB; } while (0)
#define C_COR(acc) do { _Pragma("unroll") for (int n = 0; n < 6; ++n) acc[n] *= mu2; PIN6(acc); asm volatile("s_nop 1"); SB; } while (0)
#define C_ST2B() do { var2 = __builtin_fmaxf(var2 * (1.0f / 640.0f) - mu2 * mu2, 0.f) + LN_EPS; rs2 = __builtin_amdgcn_rsqf(var2); \
        k0 = rs2 * -2.3022082f; k1 = rs2 * rs2 * rs2 * -0.10294324f; asm volatile("" : "+v"(k0), "+v"(k1), "+v"(rs2), "+v"(var2)); SB; } while (0)
#define C_EPI_E1() do { _Pragma("unroll") for (int n = 0; n < 6; ++n) accE[n] = gelu4(accG[n] * mu2 + accE[n], k0, k1); \
        STATS(accE, 6, s1_, s2_); \
        STG((v4u{__builtin_bit_cast(u32, s1_), (u32)tag, __builtin_bit_cast(u32, s2_), (u32)tag ^ (u32)(tag >> 32)}), e0, 0); SB; } while (0)
        v4u ga_, gb_;
#define go_ (sPQ + G3_IN + q * 512 + r * 16)
#define C_G3_ASK() do { ga_ = LDG(go_, 0); gb_ = LDG(go_ + 256, 0); SB; } while (0)
#define G3_BAD() ({ u32 x_ = (ga_.y ^ (u32)tag) | (ga_.w ^ t2_), y_ = (gb_.y ^ (u32)tag) | (gb_.w ^ t2_); asm volatile("" : "+v"(x_), "+v"(y_)); __ballot((x_ | y_) != 0) != 0; })
#define C_EPI_E2() do { const u32 t2_ = (u32)tag ^ (u32)(tag >> 32); \
        if (__builtin_expect(G3_BAD(), 0)) { int n_ = SPIN_CAP; _Pragma("unroll 1") do { \
            asm volatile("buffer_load_dwordx4 %0, %2, %3, 0 offen sc1\n buffer_load_dwordx4 %1, %2, %3, 0 offen offset:256 sc1\n s_waitcnt vmcnt(0)" \
                         : "=&v"(ga_), "=&v"(gb_) : "v"(go_), "s"(sw) : "memory"); } while (G3_BAD() && --n_); } \
        float mu3_ = F_(ga_.x) + F_(gb_.x), rs3_ = F_(ga_.z) + F_(gb_.z); sum_q2(mu3_, rs3_); mu3_ *= 1.0f / 768.0f; \
        rs3_ = __builtin_amdgcn_rsqf(__builtin_fmaxf(rs3_ * (1.0f / 768.0f) - mu3_ * mu3_, 0.f) + LN_EPS * var2); mu3_ *= -rs3_; \
        _Pragma("unroll") for (int n = 0; n < 6; ++n) { const f4 z_ = accE[n] * rs3_ + mu3_; \
            *(LAS v2u*)(e1 + n * 32) = v2u{pk(z_[0], z_[1]), pk(z_[2], z_[3])}; } \
        asm volatile("" ::: "memory"); v2u lo_[3], hi_[3]; \
        _Pragma("unroll") for (int k = 0; k < 3; ++k) { const u32 ad_ = e2 + k * 64; lo_[k] = trd(ad_); hi_[k] = trd(ad_ + 800); } \
        _Pragma("unroll") for (int k = 0; k < 3; ++k) \
            STG((v4u{lo_[k].x, lo_[k].y, hi_[k].x, hi_[k].y}), e3, k * 2048); \
        SB; } while (0)
        v4u sa[NSTG];
#define C_PT_RD(k, base) do { const u32 a_ = (base) + r * 128 + ((q ^ (r >> 1)) << 4); sa[2 * k] = LDS128(a_, 0); sa[2 * k + 1] = LDS128(a_ ^ 64, 0); asm volatile("" ::: "memory"); SB; } while (0)
#define C_EPI_G() do { _Pragma("unroll") for (int n = 0; n < 6; ++n) { accG[n] = gelu4(accG[n], k0, k1) * rs2; accH[0][n] = f4{bias, bias, bias, bias}; } PIN6(accG); PIN6(accH[0]); } while (0)
#define C_EPI_H() do { _Pragma("unroll") for (int k = 0; k < 3; ++k) { \
            const f4 u0_ = accG[2 * k] * accH[0][2 * k], u1_ = accG[2 * k + 1] * accH[0][2 * k + 1]; \
            uf[k] = PK4(u0_, u1_); STG(uf[k], e0, k * 1024); SB; } } while (0)
#define C_EPI_O() do { const __amdgpu_buffer_rsrc_t ro_ = __builtin_amdgcn_make_buffer_rsrc(out, 0, 2048 * 1280, 0x00020000); \
        const v4u o0_ = bperm4(e2, PK4(accO[0], accO[1])), o1_ = bperm4(e2, PK4(accO[2], accO[3])); \
        const v2u o2_ = bperm2(e2, v2u{pk(accO[4][0], accO[4][1]), pk(accO[4][2], accO[4][3])}); \
        __builtin_amdgcn_raw_buffer_store_b128(o0_, ro_, e0, 0, 0); __builtin_amdgcn_raw_buffer_store_b128(o1_, ro_, e0 + 64, 0, 0); \
        __builtin_amdgcn_raw_buffer_store_b64(o2_, ro_, e1, 0, 0); } while (0)
        __builtin_amdgcn_s_sleep(2); C_RT_LD(); C_X_LD();
        SET_SWZ(w * 4096u); C_RT_WR(); BAR(); SB;
        C_RT_RD(); C_RT_ST(0); C_RT_ST(1); C_RT_ST(2); C_RT_CS(); C_RT_ST(3); C_X_FIX();
        SKIP(612);
        f4 accA[2][5], accD[5], accE[6], accG[6], accH[1][6], accO[5]; v4u pq[40], hf[20], uf[24];
        NEW_LIN();
        BAR(); SB;
        const v4u aa_0_0_0 = LDS128(swz0[1], 40960); SB;
        const v4u aa_0_1_0 = LDS128(swz1[1], 40960); SB;
        const v4u ba_0_0_0 = LDS128(lin[1], 57344); SB;
        const v4u ba_0_1_0 = LDS128(lin[1], 62464); SB;
        const v4u aa_0_0_1 = LDS128(swz0[1], 43008); SB;
        const v4u aa_0_1_1 = LDS128(swz1[1], 43008); SB;
        const v4u ba_0_0_1 = LDS128(lin[1], 58368); SB;
        const v4u ba_0_1_1 = LDS128(lin[1], 63488); SB;
        WAITL(2); SB;
        MFMA0(accA[0][0], ba_0_0_0, aa_0_0_0); SB;
        const v4u ba_0_0_2 = LDS128(lin[1], 59392); SB;
        MFMA(accA[0][0], ba_0_1_0, aa_0_1_0); SB;
        const v4u ba_0_1_2 = LDS128(lin[1], 64512); SB;
        MFMA0(accA[1][0], ba_0_0_0, aa_0_0_1); SB;
        const v4u ba_0_0_3 = LDS128(lin[1], 60416); SB;
        MFMA(accA[1][0], ba_0_1_0, aa_0_1_1); SB;
        WAITL(3); SB;
        MFMA0(accA[0][1], ba_0_0_1, aa_0_0_0); SB;
        const v4u ba_0_1_3 = LDS128(lin[2], 0); SB;
        MFMA(accA[0][1], ba_0_1_1, aa_0_1_0); SB;
        const v4u ba_0_0_4 = LDS128(lin[1], 61440); SB;
        MFMA0(accA[1][1], ba_0_0_1, aa_0_0_1); SB;
        MFMA(accA[1][1], ba_0_1_1, aa_0_1_1); SB;
        const v4u ba_0_1_4 = LDS128(lin[2], 1024); SB;
        WAITL(4); SB;
        MFMA0(accA[0][2], ba_0_0_2, aa_0_0_0); SB;
        BAR(); SB;
        const v4u aa_1_0_0 = LDS128(swz0[2], 2048); SB;
        MFMA(accA[0][2], ba_0_1_2, aa_0_1_0); SB;
        MFMA0(accA[1][2], ba_0_0_2, aa_0_0_1); SB;
        const v4u aa_1_1_0 = LDS128(swz1[2], 2048); SB;
        MFMA(accA[1][2], ba_0_1_2, aa_0_1_1); SB;
        const v4u ba_1_0_0 = LDS128(lin[2], 18432); SB;
        WAITL(5); SB;
        MFMA0(accA[0][3], ba_0_0_3, aa_0_0_0); SB;
        const v4u ba_1_1_0 = LDS128(lin[2], 23552); SB;
        MFMA(accA[0][3], ba_0_1_3, aa_0_1_0); SB;
        MFMA0(accA[1][3], ba_0_0_3, aa_0_0_1); SB;
        const v4u aa_1_0_1 = LDS128(swz0[2], 4096); SB;
        MFMA(accA[1][3], ba_0_1_3, aa_0_1_1); SB;
        const v4u aa_1_1_1 = LDS128(swz1[2], 4096); SB;
        WAITL(6); SB;
        MFMA0(accA[0][4], ba_0_0_4, aa_0_0_0); SB;
        MFMA(accA[0][4], ba_0_1_4, aa_0_1_0); SB;
        const v4u ba_1_0_1 = LDS128(lin[2], 19456); SB;
        MFMA0(accA[1][4], ba_0_0_4, aa_0_0_1); SB;
        const v4u ba_1_1_1 = LDS128(lin[2], 24576); SB;
        MFMA(accA[1][4], ba_0_1_4, aa_0_1_1); SB;
        WAITL(2); SB;
        MFMA(accA[0][0], ba_1_0_0, aa_1_0_0); SB;
        const v4u ba_1_0_2 = LDS128(lin[2], 20480); SB;
        MFMA(accA[0][0], ba_1_1_0, aa_1_1_0); SB;
        const v4u ba_1_1_2 = LDS128(lin[2], 25600); SB;
        MFMA(accA[1][0], ba_1_0_0, aa_1_0_1); SB;
        const v4u ba_1_0_3 = LDS128(lin[2], 21504); SB;
        MFMA(accA[1][0], ba_1_1_0, aa_1_1_1); SB;
        WAITL(3); SB;
        MFMA(accA[0][1], ba_1_0_1, aa_1_0_0); SB;
        const v4u ba_1_1_3 = LDS128(lin[2], 26624); SB;
        MFMA(accA[0][1], ba_1_1_1, aa_1_1_0); SB;
        const v4u ba_1_0_4 = LDS128(lin[2], 22528); SB;
        MFMA(accA[1][1], ba_1_0_1, aa_1_0_1); SB;
        MFMA(accA[1][1], ba_1_1_1, aa_1_1_1); SB;
        const v4u ba_1_1_4 = LDS128(lin[2], 27648); SB;
        WAITL(4); SB;
        MFMA(accA[0][2], ba_1_0_2, aa_1_0_0); SB;
        BAR(); SB;
        const v4u aa_2_0_0 = LDS128(swz0[0], 0); SB;
        MFMA(accA[0][2], ba_1_1_2, aa_1_1_0); SB;
        MFMA(accA[1][2], ba_1_0_2, aa_1_0_1); SB;
        const v4u aa_2_1_0 = LDS128(swz1[0], 0); SB;
        MFMA(accA[1][2], ba_1_1_2, aa_1_1_1); SB;
        const v4u ba_2_0_0 = LDS128(lin[0], 16384); SB;
        WAITL(5); SB;
        MFMA(accA[0][3], ba_1_0_3, aa_1_0_0); SB;
        const v4u ba_2_1_0 = LDS128(lin[0], 21504); SB;
        MFMA(accA[0][3], ba_1_1_3, aa_1_1_0); SB;
        MFMA(accA[1][3], ba_1_0_3, aa_1_0_1); SB;
        const v4u aa_2_0_1 = LDS128(swz0[0], 2048); SB;
        MFMA(accA[1][3], ba_1_1_3, aa_1_1_1); SB;
        const v4u aa_2_1_1 = LDS128(swz1[0], 2048); SB;
        WAITL(6); SB;
        MFMA(accA[0][4], ba_1_0_4, aa_1_0_0); SB;
        MFMA(accA[0][4], ba_1_1_4, aa_1_1_0); SB;
        const v4u ba_2_0_1 = LDS128(lin[0], 17408); SB;
        MFMA(accA[1][4], ba_1_0_4, aa_1_0_1); SB;
        const v4u ba_2_1_1 = LDS128(lin[0], 22528); SB;
        MFMA(accA[1][4], ba_1_1_4, aa_1_1_1); SB;
        WAITL(2); SB;
        MFMA(accA[0][0], ba_2_0_0, aa_2_0_0); SB;
        const v4u ba_2_0_2 = LDS128(lin[0], 18432); SB;
        MFMA(accA[0][0], ba_2_1_0, aa_2_1_0); SB;
        const v4u ba_2_1_2 = LDS128(lin[0], 23552); SB;
        MFMA(accA[1][0], ba_2_0_0, aa_2_0_1); SB;
        const v4u ba_2_0_3 = LDS128(lin[0], 19456); SB;
        MFMA(accA[1][0], ba_2_1_0, aa_2_1_1); SB;
        WAITL(3); SB;
        MFMA(accA[0][1], ba_2_0_1, aa_2_0_0); SB;
        const v4u ba_2_1_3 = LDS128(lin[0], 24576); SB;
        MFMA(accA[0][1], ba_2_1_1, aa_2_1_0); SB;
        const v4u ba_2_0_4 = LDS128(lin[0], 20480); SB;
        MFMA(accA[1][1], ba_2_0_1, aa_2_0_1); SB;
        MFMA(accA[1][1], ba_2_1_1, aa_2_1_1); SB;
        const v4u ba_2_1_4 = LDS128(lin[0], 25600); SB;
        WAITL(4); SB;
        MFMA(accA[0][2], ba_2_0_2, aa_2_0_0); SB;
        BAR(); SB;
        const v4u aa_3_0_0 = LDS128(swz0[0], 26624); SB;
        MFMA(accA[0][2], ba_2_1_2, aa_2_1_0); SB;
        MFMA(accA[1][2], ba_2_0_2, aa_2_0_1); SB;
        const v4u aa_3_1_0 = LDS128(swz1[0], 26624); SB;
        MFMA(accA[1][2], ba_2_1_2, aa_2_1_1); SB;
        const v4u ba_3_0_0 = LDS128(lin[0], 43008); SB;
        WAITL(5); SB;
        MFMA(accA[0][3], ba_2_0_3, aa_2_0_0); SB;
        const v4u ba_3_1_0 = LDS128(lin[0], 48128); SB;
        MFMA(accA[0][3], ba_2_1_3, aa_2_1_0); SB;
        MFMA(accA[1][3], ba_2_0_3, aa_2_0_1); SB;
        const v4u aa_3_0_1 = LDS128(swz0[0], 28672); SB;
        MFMA(accA[1][3], ba_2_1_3, aa_2_1_1); SB;
        const v4u aa_3_1_1 = LDS128(swz1[0], 28672); SB;
        WAITL(6); SB;
        MFMA(accA[0][4], ba_2_0_4, aa_2_0_0); SB;
        MFMA(accA[0][4], ba_2_1_4, aa_2_1_0); SB;
        const v4u ba_3_0_1 = LDS128(lin[0], 44032); SB;
        MFMA(accA[1][4], ba_2_0_4, aa_2_0_1); SB;
        const v4u ba_3_1_1 = LDS128(lin[0], 49152); SB;
        MFMA(accA[1][4], ba_2_1_4, aa_2_1_1); SB;
        WAITL(2); SB;
        MFMA(accA[0][0], ba_3_0_0, aa_3_0_0); SB;
        const v4u ba_3_0_2 = LDS128(lin[0], 45056); SB;
        MFMA(accA[0][0], ba_3_1_0, aa_3_1_0); SB;
        const v4u ba_3_1_2 = LDS128(lin[0], 50176); SB;
        MFMA(accA[1][0], ba_3_0_0, aa_3_0_1); SB;
        const v4u ba_3_0_3 = LDS128(lin[0], 46080); SB;
        MFMA(accA[1][0], ba_3_1_0, aa_3_1_1); SB;
        WAITL(3); SB;
        MFMA(accA[0][1], ba_3_0_1, aa_3_0_0); SB;
        const v4u ba_3_1_3 = LDS128(lin[0], 51200); SB;
        MFMA(accA[0][1], ba_3_1_1, aa_3_1_0); SB;
        const v4u ba_3_0_4 = LDS128(lin[0], 47104); SB;
        MFMA(accA[1][1], ba_3_0_1, aa_3_0_1); SB;
        MFMA(accA[1][1], ba_3_1_1, aa_3_1_1); SB;
        const v4u ba_3_1_4 = LDS128(lin[0], 52224); SB;
        WAITL(4); SB;
        MFMA(accA[0][2], ba_3_0_2, aa_3_0_0); SB;
        BAR(); SB;
        DRAIN();
        const v4u aa_4_0_0 = LDS128(swz0[0], 53248); SB;
        MFMA(accA[0][2], ba_3_1_2, aa_3_1_0); SB;
        MFMA(accA[1][2], ba_3_0_2, aa_3_0_1); SB;
        const v4u aa_4_1_0 = LDS128(swz1[0], 53248); SB;
        MFMA(accA[1][2], ba_3_1_2, aa_3_1_1); SB;
        const v4u ba_4_0_0 = LDS128(lin[1], 4096); SB;
        WAITL(5); SB;
        MFMA(accA[0][3], ba_3_0_3, aa_3_0_0); SB;
        const v4u ba_4_1_0 = LDS128(lin[1], 9216); SB;
        MFMA(accA[0][3], ba_3_1_3, aa_3_1_0); SB;
        MFMA(accA[1][3], ba_3_0_3, aa_3_0_1); SB;
        const v4u aa_4_0_1 = LDS128(swz0[0], 55296); SB;
        MFMA(accA[1][3], ba_3_1_3, aa_3_1_1); SB;
        const v4u aa_4_1_1 = LDS128(swz1[0], 55296); SB;
        WAITL(6); SB;
        MFMA(accA[0][4], ba_3_0_4, aa_3_0_0); SB;
        MFMA(accA[0][4], ba_3_1_4, aa_3_1_0); SB;
        const v4u ba_4_0_1 = LDS128(lin[1], 5120); SB;
        MFMA(accA[1][4], ba_3_0_4, aa_3_0_1); SB;
        const v4u ba_4_1_1 = LDS128(lin[1], 10240); SB;
        MFMA(accA[1][4], ba_3_1_4, aa_3_1_1); SB;
        WAITL(2); SB;
        MFMA(accA[0][0], ba_4_0_0, aa_4_0_0); SB;
        const v4u ba_4_0_2 = LDS128(lin[1], 6144); SB;
        MFMA(accA[0][0], ba_4_1_0, aa_4_1_0); SB;
        const v4u ba_4_1_2 = LDS128(lin[1], 11264); SB;
        MFMA(accA[1][0], ba_4_0_0, aa_4_0_1); SB;
        const v4u ba_4_0_3 = LDS128(lin[1], 7168); SB;
        MFMA(accA[1][0], ba_4_1_0, aa_4_1_1); SB;
        WAITL(3); SB;
        MFMA(accA[0][1], ba_4_0_1, aa_4_0_0); SB;
        const v4u ba_4_1_3 = LDS128(lin[1], 12288); SB;
        MFMA(accA[0][1], ba_4_1_1, aa_4_1_0); SB;
        const v4u ba_4_0_4 = LDS128(lin[1], 8192); SB;
        MFMA(accA[1][1], ba_4_0_1, aa_4_0_1); SB;
        MFMA(accA[1][1], ba_4_1_1, aa_4_1_1); SB;
        const v4u ba_4_1_4 = LDS128(lin[1], 13312); SB;
        WAITL(4); SB;
        MFMA(accA[0][2], ba_4_0_2, aa_4_0_0); SB;
        BAR(); SB;
        C_FLAG_FB();
        const v4u aa_5_0_0 = LDS128(swz0[1], 14336); SB;
        MFMA(accA[0][2], ba_4_1_2, aa_4_1_0); SB;
        MFMA(accA[1][2], ba_4_0_2, aa_4_0_1); SB;
        const v4u aa_5_1_0 = LDS128(swz1[1], 14336); SB;
        MFMA(accA[1][2], ba_4_1_2, aa_4_1_1); SB;
        const v4u ba_5_0_0 = LDS128(lin[1], 30720); SB;
        WAITL(5); SB;
        MFMA(accA[0][3], ba_4_0_3, aa_4_0_0); SB;
        const v4u ba_5_1_0 = LDS128(lin[1], 35840); SB;
        MFMA(accA[0][3], ba_4_1_3, aa_4_1_0); SB;
        MFMA(accA[1][3], ba_4_0_3, aa_4_0_1); SB;
        const v4u aa_5_0_1 = LDS128(swz0[1], 16384); SB;
        MFMA(accA[1][3], ba_4_1_3, aa_4_1_1); SB;
        const v4u aa_5_1_1 = LDS128(swz1[1], 16384); SB;
        WAITL(6); SB;
        MFMA(accA[0][4], ba_4_0_4, aa_4_0_0); SB;
        MFMA(accA[0][4], ba_4_1_4, aa_4_1_0); SB;
        const v4u ba_5_0_1 = LDS128(lin[1], 31744); SB;
        MFMA(accA[1][4], ba_4_0_4, aa_4_0_1); SB;
        const v4u ba_5_1_1 = LDS128(lin[1], 36864); SB;
        MFMA(accA[1][4], ba_4_1_4, aa_4_1_1); SB;
        WAITL(2); SB;
        MFMA(accA[0][0], ba_5_0_0, aa_5_0_0); SB;
        const v4u ba_5_0_2 = LDS128(lin[1], 32768); SB;
        MFMA(accA[0][0], ba_5_1_0, aa_5_1_0); SB;
        const v4u ba_5_1_2 = LDS128(lin[1], 37888); SB;
        MFMA(accA[1][0], ba_5_0_0, aa_5_0_1); SB;
        const v4u ba_5_0_3 = LDS128(lin[1], 33792); SB;
        MFMA(accA[1][0], ba_5_1_0, aa_5_1_1); SB;
        WAITL(3); SB;
        MFMA(accA[0][1], ba_5_0_1, aa_5_0_0); SB;
        const v4u ba_5_1_3 = LDS128(lin[1], 38912); SB;
        MFMA(accA[0][1], ba_5_1_1, aa_5_1_0); SB;
        const v4u ba_5_0_4 = LDS128(lin[1], 34816); SB;
        MFMA(accA[1][1], ba_5_0_1, aa_5_0_1); SB;
        MFMA(accA[1][1], ba_5_1_1, aa_5_1_1); SB;
        const v4u ba_5_1_4 = LDS128(lin[1], 39936); SB;
        WAITL(4); SB;
        MFMA(accA[0][2], ba_5_0_2, aa_5_0_0); SB;
        BAR(); SB;
        const v4u aa_6_0_0 = LDS128(swz0[1], 40960); SB;
        MFMA(accA[0][2], ba_5_1_2, aa_5_1_0); SB;
        MFMA(accA[1][2], ba_5_0_2, aa_5_0_1); SB;
        const v4u aa_6_1_0 = LDS128(swz1[1], 40960); SB;
        MFMA(accA[1][2], ba_5_1_2, aa_5_1_1); SB;
        const v4u ba_6_0_0 = LDS128(lin[1], 57344); SB;
        WAITL(5); SB;
        MFMA(accA[0][3], ba_5_0_3, aa_5_0_0); SB;
        const v4u ba_6_1_0 = LDS128(lin[1], 62464); SB;
        MFMA(accA[0][3], ba_5_1_3, aa_5_1_0); SB;
        MFMA(accA[1][3], ba_5_0_3, aa_5_0_1); SB;
        const v4u aa_6_0_1 = LDS128(swz0[1], 43008); SB;
        MFMA(accA[1][3], ba_5_1_3, aa_5_1_1); SB;
        const v4u aa_6_1_1 = LDS128(swz1[1], 43008); SB;
        WAITL(6); SB;
        MFMA(accA[0][4], ba_5_0_4, aa_5_0_0); SB;
        MFMA(accA[0][4], ba_5_1_4, aa_5_1_0); SB;
        const v4u ba_6_0_1 = LDS128(lin[1], 58368); SB;
        MFMA(accA[1][4], ba_5_0_4, aa_5_0_1); SB;
        const v4u ba_6_1_1 = LDS128(lin[1], 63488); SB;
        MFMA(accA[1][4], ba_5_1_4, aa_5_1_1); SB;
        WAITL(2); SB;
        MFMA(accA[0][0], ba_6_0_0, aa_6_0_0); SB;
        const v4u ba_6_0_2 = LDS128(lin[1], 59392); SB;
        MFMA(accA[0][0], ba_6_1_0, aa_6_1_0); SB;
        const v4u ba_6_1_2 = LDS128(lin[1], 64512); SB;
        MFMA(accA[1][0], ba_6_0_0, aa_6_0_1); SB;
        const v4u ba_6_0_3 = LDS128(lin[1], 60416); SB;
        MFMA(accA[1][0], ba_6_1_0, aa_6_1_1); SB;
        WAITL(3); SB;
        MFMA(accA[0][1], ba_6_0_1, aa_6_0_0); SB;
        const v4u ba_6_1_3 = LDS128(lin[2], 0); SB;
        MFMA(accA[0][1], ba_6_1_1, aa_6_1_0); SB;
        const v4u ba_6_0_4 = LDS128(lin[1], 61440); SB;
        MFMA(accA[1][1], ba_6_0_1, aa_6_0_1); SB;
        MFMA(accA[1][1], ba_6_1_1, aa_6_1_1); SB;
        const v4u ba_6_1_4 = LDS128(lin[2], 1024); SB;
        WAITL(4); SB;
        MFMA(accA[0][2], ba_6_0_2, aa_6_0_0); SB;
        BAR(); SB;
        const v4u aa_7_0_0 = LDS128(swz0[2], 2048); SB;
        MFMA(accA[0][2], ba_6_1_2, aa_6_1_0); SB;
        MFMA(accA[1][2], ba_6_0_2, aa_6_0_1); SB;
        const v4u aa_7_1_0 = LDS128(swz1[2], 2048); SB;
        MFMA(accA[1][2], ba_6_1_2, aa_6_1_1); SB;
        const v4u ba_7_0_0 = LDS128(lin[2], 18432); SB;
        WAITL(5); SB;
        MFMA(accA[0][3], ba_6_0_3, aa_6_0_0); SB;
        const v4u ba_7_1_0 = LDS128(lin[2], 23552); SB;
        MFMA(accA[0][3], ba_6_1_3, aa_6_1_0); SB;
        MFMA(accA[1][3], ba_6_0_3, aa_6_0_1); SB;
        const v4u aa_7_0_1 = LDS128(swz0[2], 4096); SB;
        MFMA(accA[1][3], ba_6_1_3, aa_6_1_1); SB;
        const v4u aa_7_1_1 = LDS128(swz1[2], 4096); SB;
        WAITL(6); SB;
        MFMA(accA[0][4], ba_6_0_4, aa_6_0_0); SB;
        MFMA(accA[0][4], ba_6_1_4, aa_6_1_0); SB;
        const v4u ba_7_0_1 = LDS128(lin[2], 19456); SB;
        MFMA(accA[1][4], ba_6_0_4, aa_6_0_1); SB;
        const v4u ba_7_1_1 = LDS128(lin[2], 24576); SB;
        MFMA(accA[1][4], ba_6_1_4, aa_6_1_1); SB;
        WAITL(2); SB;
        MFMA(accA[0][0], ba_7_0_0, aa_7_0_0); SB;
        const v4u ba_7_0_2 = LDS128(lin[2], 20480); SB;
        MFMA(accA[0][0], ba_7_1_0, aa_7_1_0); SB;
        const v4u ba_7_1_2 = LDS128(lin[2], 25600); SB;
        MFMA(accA[1][0], ba_7_0_0, aa_7_0_1); SB;
        const v4u ba_7_0_3 = LDS128(lin[2], 21504); SB;
        MFMA(accA[1][0], ba_7_1_0, aa_7_1_1); SB;
        WAITL(3); SB;
        MFMA(accA[0][1], ba_7_0_1, aa_7_0_0); SB;
        const v4u ba_7_1_3 = LDS128(lin[2], 26624); SB;
        MFMA(accA[0][1], ba_7_1_1, aa_7_1_0); SB;
        const v4u ba_7_0_4 = LDS128(lin[2], 22528); SB;
        MFMA(accA[1][1], ba_7_0_1, aa_7_0_1); SB;
        MFMA(accA[1][1], ba_7_1_1, aa_7_1_1); SB;
        const v4u ba_7_1_4 = LDS128(lin[2], 27648); SB;
        WAITL(4); SB;
        MFMA(accA[0][2], ba_7_0_2, aa_7_0_0); SB;
        BAR(); SB;
        const v4u aa_8_0_0 = LDS128(swz0[0], 0); SB;
        MFMA(accA[0][2], ba_7_1_2, aa_7_1_0); SB;
        MFMA(accA[1][2], ba_7_0_2, aa_7_0_1); SB;
        const v4u aa_8_1_0 = LDS128(swz1[0], 0); SB;
        MFMA(accA[1][2], ba_7_1_2, aa_7_1_1); SB;
        const v4u ba_8_0_0 = LDS128(lin[0], 16384); SB;
        WAITL(5); SB;
        MFMA(accA[0][3], ba_7_0_3, aa_7_0_0); SB;
        const v4u ba_8_1_0 = LDS128(lin[0], 21504); SB;
        MFMA(accA[0][3], ba_7_1_3, aa_7_1_0); SB;
        MFMA(accA[1][3], ba_7_0_3, aa_7_0_1); SB;
        const v4u aa_8_0_1 = LDS128(swz0[0], 2048); SB;
        MFMA(accA[1][3], ba_7_1_3, aa_7_1_1); SB;
        const v4u aa_8_1_1 = LDS128(swz1[0], 2048); SB;
        WAITL(6); SB;
        MFMA(accA[0][4], ba_7_0_4, aa_7_0_0); SB;
        MFMA(accA[0][4], ba_7_1_4, aa_7_1_0); SB;
        const v4u ba_8_0_1 = LDS128(lin[0], 17408); SB;
        MFMA(accA[1][4], ba_7_0_4, aa_7_0_1); SB;
        const v4u ba_8_1_1 = LDS128(lin[0], 22528); SB;
        MFMA(accA[1][4], ba_7_1_4, aa_7_1_1); SB;
        WAITL(2); SB;
        MFMA(accA[0][0], ba_8_0_0, aa_8_0_0); SB;
        const v4u ba_8_0_2 = LDS128(lin[0], 18432); SB;
        MFMA(accA[0][0], ba_8_1_0, aa_8_1_0); SB;
        const v4u ba_8_1_2 = LDS128(lin[0], 23552); SB;
        MFMA(accA[1][0], ba_8_0_0, aa_8_0_1); SB;
        const v4u ba_8_0_3 = LDS128(lin[0], 19456); SB;
        MFMA(accA[1][0], ba_8_1_0, aa_8_1_1); SB;
        WAITL(3); SB;
        MFMA(accA[0][1], ba_8_0_1, aa_8_0_0); SB;
        const v4u ba_8_1_3 = LDS128(lin[0], 24576); SB;
        MFMA(accA[0][1], ba_8_1_1, aa_8_1_0); SB;
        const v4u ba_8_0_4 = LDS128(lin[0], 20480); SB;
        MFMA(accA[1][1], ba_8_0_1, aa_8_0_1); SB;
        MFMA(accA[1][1], ba_8_1_1, aa_8_1_1); SB;
        const v4u ba_8_1_4 = LDS128(lin[0], 25600); SB;
        WAITL(4); SB;
        MFMA(accA[0][2], ba_8_0_2, aa_8_0_0); SB;
        BAR(); SB;
        const v4u aa_9_0_0 = LDS128(swz0[0], 26624); SB;
        MFMA(accA[0][2], ba_8_1_2, aa_8_1_0); SB;
        MFMA(accA[1][2], ba_8_0_2, aa_8_0_1); SB;
        const v4u aa_9_1_0 = LDS128(swz1[0], 26624); SB;
        MFMA(accA[1][2], ba_8_1_2, aa_8_1_1); SB;
        const v4u ba_9_0_0 = LDS128(lin[0], 43008); SB;
        WAITL(5); SB;
        MFMA(accA[0][3], ba_8_0_3, aa_8_0_0); SB;
        const v4u ba_9_1_0 = LDS128(lin[0], 48128); SB;
        MFMA(accA[0][3], ba_8_1_3, aa_8_1_0); SB;
        MFMA(accA[1][3], ba_8_0_3, aa_8_0_1); SB;
        const v4u aa_9_0_1 = LDS128(swz0[0], 28672); SB;
        MFMA(accA[1][3], ba_8_1_3, aa_8_1_1); SB;
        const v4u aa_9_1_1 = LDS128(swz1[0], 28672); SB;
        WAITL(6); SB;
        MFMA(accA[0][4], ba_8_0_4, aa_8_0_0); SB;
        MFMA(accA[0][4], ba_8_1_4, aa_8_1_0); SB;
        const v4u ba_9_0_1 = LDS128(lin[0], 44032); SB;
        MFMA(accA[1][4], ba_8_0_4, aa_8_0_1); SB;
        const v4u ba_9_1_1 = LDS128(lin[0], 49152); SB;
        MFMA(accA[1][4], ba_8_1_4, aa_8_1_1); SB;
        WAITL(2); SB;
        MFMA(accA[0][0], ba_9_0_0, aa_9_0_0); SB;
        const v4u ba_9_0_2 = LDS128(lin[0], 45056); SB;
        MFMA(accA[0][0], ba_9_1_0, aa_9_1_0); SB;
        const v4u ba_9_1_2 = LDS128(lin[0], 50176); SB;
        MFMA(accA[1][0], ba_9_0_0, aa_9_0_1); SB;
        const v4u ba_9_0_3 = LDS128(lin[0], 46080); SB;
        MFMA(accA[1][0], ba_9_1_0, aa_9_1_1); SB;
        WAITL(3); SB;
        MFMA(accA[0][1], ba_9_0_1, aa_9_0_0); SB;
        const v4u ba_9_1_3 = LDS128(lin[0], 51200); SB;
        MFMA(accA[0][1], ba_9_1_1, aa_9_1_0); SB;
        const v4u ba_9_0_4 = LDS128(lin[0], 47104); SB;
        MFMA(accA[1][1], ba_9_0_1, aa_9_0_1); SB;
        MFMA(accA[1][1], ba_9_1_1, aa_9_1_1); SB;
        const v4u ba_9_1_4 = LDS128(lin[0], 52224); SB;
        WAITL(4); SB;
        MFMA(accA[0][2], ba_9_0_2, aa_9_0_0); SB;
        BAR(); SB;
        const v4u aa_10_0_0 = LDS128(swz0[0], 53248); SB;
        MFMA(accA[0][2], ba_9_1_2, aa_9_1_0); SB;
        MFMA(accA[1][2], ba_9_0_2, aa_9_0_1); SB;
        const v4u aa_10_1_0 = LDS128(swz1[0], 53248); SB;
        MFMA(accA[1][2], ba_9_1_2, aa_9_1_1); SB;
        const v4u ba_10_0_0 = LDS128(lin[1], 4096); SB;
        WAITL(5); SB;
        MFMA(accA[0][3], ba_9_0_3, aa_9_0_0); SB;
        const v4u ba_10_1_0 = LDS128(lin[1], 9216); SB;
        MFMA(accA[0][3], ba_9_1_3, aa_9_1_0); SB;
        MFMA(accA[1][3], ba_9_0_3, aa_9_0_1); SB;
        const v4u aa_10_0_1 = LDS128(swz0[0], 55296); SB;
        MFMA(accA[1][3], ba_9_1_3, aa_9_1_1); SB;
        const v4u aa_10_1_1 = LDS128(swz1[0], 55296); SB;
        WAITL(6); SB;
        MFMA(accA[0][4], ba_9_0_4, aa_9_0_0); SB;
        MFMA(accA[0][4], ba_9_1_4, aa_9_1_0); SB;
        const v4u ba_10_0_1 = LDS128(lin[1], 5120); SB;
        MFMA(accA[1][4], ba_9_0_4, aa_9_0_1); SB;
        const v4u ba_10_1_1 = LDS128(lin[1], 10240); SB;
        MFMA(accA[1][4], ba_9_1_4, aa_9_1_1); SB;
        WAITL(2); SB;
        MFMA(accA[0][0], ba_10_0_0, aa_10_0_0); SB;
        const v4u ba_10_0_2 = LDS128(lin[1], 6144); SB;
        MFMA(accA[0][0], ba_10_1_0, aa_10_1_0); SB;
        const v4u ba_10_1_2 = LDS128(lin[1], 11264); SB;
        MFMA(accA[1][0], ba_10_0_0, aa_10_0_1); SB;
        const v4u ba_10_0_3 = LDS128(lin[1], 7168); SB;
        MFMA(accA[1][0], ba_10_1_0, aa_10_1_1); SB;
        WAITL(3); SB;
        MFMA(accA[0][1], ba_10_0_1, aa_10_0_0); SB;
        const v4u ba_10_1_3 = LDS128(lin[1], 12288); SB;
        MFMA(accA[0][1], ba_10_1_1, aa_10_1_0); SB;
        const v4u ba_10_0_4 = LDS128(lin[1], 8192); SB;
        MFMA(accA[1][1], ba_10_0_1, aa_10_0_1); SB;
        MFMA(accA[1][1], ba_10_1_1, aa_10_1_1); SB;
        const v4u ba_10_1_4 = LDS128(lin[1], 13312); SB;
        WAITL(4); SB;
        MFMA(accA[0][2], ba_10_0_2, aa_10_0_0); SB;
        BAR(); SB;
        const v4u aa_11_0_0 = LDS128(swz0[1], 14336); SB;
        MFMA(accA[0][2], ba_10_1_2, aa_10_1_0); SB;
        MFMA(accA[1][2], ba_10_0_2, aa_10_0_1); SB;
        const v4u aa_11_1_0 = LDS128(swz1[1], 14336); SB;
        MFMA(accA[1][2], ba_10_1_2, aa_10_1_1); SB;
        const v4u ba_11_0_0 = LDS128(lin[1], 30720); SB;
        WAITL(5); SB;
        MFMA(accA[0][3], ba_10_0_3, aa_10_0_0); SB;
        const v4u ba_11_1_0 = LDS128(lin[1], 35840); SB;
        MFMA(accA[0][3], ba_10_1_3, aa_10_1_0); SB;
        MFMA(accA[1][3], ba_10_0_3, aa_10_0_1); SB;
        const v4u aa_11_0_1 = LDS128(swz0[1], 16384); SB;
        MFMA(accA[1][3], ba_10_1_3, aa_10_1_1); SB;
        const v4u aa_11_1_1 = LDS128(swz1[1], 16384); SB;
        WAITL(6); SB;
        MFMA(accA[0][4], ba_10_0_4, aa_10_0_0); SB;
        MFMA(accA[0][4], ba_10_1_4, aa_10_1_0); SB;
        const v4u ba_11_0_1 = LDS128(lin[1], 31744); SB;
        MFMA(accA[1][4], ba_10_0_4, aa_10_0_1); SB;
        const v4u ba_11_1_1 = LDS128(lin[1], 36864); SB;
        MFMA(accA[1][4], ba_10_1_4, aa_10_1_1); SB;
        WAITL(2); SB;
        MFMA(accA[0][0], ba_11_0_0, aa_11_0_0); SB;
        const v4u ba_11_0_2 = LDS128(lin[1], 32768); SB;
        MFMA(accA[0][0], ba_11_1_0, aa_11_1_0); SB;
        const v4u ba_11_1_2 = LDS128(lin[1], 37888); SB;
        MFMA(accA[1][0], ba_11_0_0, aa_11_0_1); SB;
        const v4u ba_11_0_3 = LDS128(lin[1], 33792); SB;
        MFMA(accA[1][0], ba_11_1_0, aa_11_1_1); SB;
        WAITL(3); SB;
        MFMA(accA[0][1], ba_11_0_1, aa_11_0_0); SB;
        const v4u ba_11_1_3 = LDS128(lin[1], 38912); SB;
        MFMA(accA[0][1], ba_11_1_1, aa_11_1_0); SB;
        const v4u ba_11_0_4 = LDS128(lin[1], 34816); SB;
        MFMA(accA[1][1], ba_11_0_1, aa_11_0_1); SB;
        MFMA(accA[1][1], ba_11_1_1, aa_11_1_1); SB;
        const v4u ba_11_1_4 = LDS128(lin[1], 39936); SB;
        WAITL(4); SB;
        MFMA(accA[0][2], ba_11_0_2, aa_11_0_0); SB;
        BAR(); SB;
        const v4u aa_12_0_0 = LDS128(swz0[1], 40960); SB;
        MFMA(accA[0][2], ba_11_1_2, aa_11_1_0); SB;
        MFMA(accA[1][2], ba_11_0_2, aa_11_0_1); SB;
        const v4u aa_12_1_0 = LDS128(swz1[1], 40960); SB;
        MFMA(accA[1][2], ba_11_1_2, aa_11_1_1); SB;
        const v4u ba_12_0_0 = LDS128(lin[1], 57344); SB;
        WAITL(5); SB;
        MFMA(accA[0][3], ba_11_0_3, aa_11_0_0); SB;
        const v4u ba_12_1_0 = LDS128(lin[1], 62464); SB;
        MFMA(accA[0][3], ba_11_1_3, aa_11_1_0); SB;
        MFMA(accA[1][3], ba_11_0_3, aa_11_0_1); SB;
        const v4u aa_12_0_1 = LDS128(swz0[1], 43008); SB;
        MFMA(accA[1][3], ba_11_1_3, aa_11_1_1); SB;
        const v4u aa_12_1_1 = LDS128(swz1[1], 43008); SB;
        WAITL(6); SB;
        MFMA(accA[0][4], ba_11_0_4, aa_11_0_0); SB;
        MFMA(accA[0][4], ba_11_1_4, aa_11_1_0); SB;
        const v4u ba_12_0_1 = LDS128(lin[1], 58368); SB;
        MFMA(accA[1][4], ba_11_0_4, aa_11_0_1); SB;
        const v4u ba_12_1_1 = LDS128(lin[1], 63488); SB;
        MFMA(accA[1][4], ba_11_1_4, aa_11_1_1); SB;
        WAITL(2); SB;
        MFMA(accA[0][0], ba_12_0_0, aa_12_0_0); SB;
        const v4u ba_12_0_2 = LDS128(lin[1], 59392); SB;
        MFMA(accA[0][0], ba_12_1_0, aa_12_1_0); SB;
        const v4u ba_12_1_2 = LDS128(lin[1], 64512); SB;
        MFMA(accA[1][0], ba_12_0_0, aa_12_0_1); SB;
        const v4u ba_12_0_3 = LDS128(lin[1], 60416); SB;
        MFMA(accA[1][0], ba_12_1_0, aa_12_1_1); SB;
        WAITL(3); SB;
        MFMA(accA[0][1], ba_12_0_1, aa_12_0_0); SB;
        const v4u ba_12_1_3 = LDS128(lin[2], 0); SB;
        MFMA(accA[0][1], ba_12_1_1, aa_12_1_0); SB;
        const v4u ba_12_0_4 = LDS128(lin[1], 61440); SB;
        MFMA(accA[1][1], ba_12_0_1, aa_12_0_1); SB;
        MFMA(accA[1][1], ba_12_1_1, aa_12_1_1); SB;
        const v4u ba_12_1_4 = LDS128(lin[2], 1024); SB;
        WAITL(4); SB;
        MFMA(accA[0][2], ba_12_0_2, aa_12_0_0); SB;
        BAR(); SB;
        const v4u aa_13_0_0 = LDS128(swz0[2], 2048); SB;
        MFMA(accA[0][2], ba_12_1_2, aa_12_1_0); SB;
        MFMA(accA[1][2], ba_12_0_2, aa_12_0_1); SB;
        const v4u aa_13_1_0 = LDS128(swz1[2], 2048); SB;
        MFMA(accA[1][2], ba_12_1_2, aa_12_1_1); SB;
        const v4u ba_13_0_0 = LDS128(lin[2], 18432); SB;
        WAITL(5); SB;
        MFMA(accA[0][3], ba_12_0_3, aa_12_0_0); SB;
        const v4u ba_13_1_0 = LDS128(lin[2], 23552); SB;
        MFMA(accA[0][3], ba_12_1_3, aa_12_1_0); SB;
        MFMA(accA[1][3], ba_12_0_3, aa_12_0_1); SB;
        const v4u aa_13_0_1 = LDS128(swz0[2], 4096); SB;
        MFMA(accA[1][3], ba_12_1_3, aa_12_1_1); SB;
        const v4u aa_13_1_1 = LDS128(swz1[2], 4096); SB;
        WAITL(6); SB;
        MFMA(accA[0][4], ba_12_0_4, aa_12_0_0); SB;
        MFMA(accA[0][4], ba_12_1_4, aa_12_1_0); SB;
        const v4u ba_13_0_1 = LDS128(lin[2], 19456); SB;
        MFMA(accA[1][4], ba_12_0_4, aa_12_0_1); SB;
        const v4u ba_13_1_1 = LDS128(lin[2], 24576); SB;
        MFMA(accA[1][4], ba_12_1_4, aa_12_1_1); SB;
        WAITL(2); SB;
        MFMA(accA[0][0], ba_13_0_0, aa_13_0_0); SB;
        const v4u ba_13_0_2 = LDS128(lin[2], 20480); SB;
        MFMA(accA[0][0], ba_13_1_0, aa_13_1_0); SB;
        const v4u ba_13_1_2 = LDS128(lin[2], 25600); SB;
        MFMA(accA[1][0], ba_13_0_0, aa_13_0_1); SB;
        const v4u ba_13_0_3 = LDS128(lin[2], 21504); SB;
        MFMA(accA[1][0], ba_13_1_0, aa_13_1_1); SB;
        WAITL(3); SB;
        MFMA(accA[0][1], ba_13_0_1, aa_13_0_0); SB;
        const v4u ba_13_1_3 = LDS128(lin[2], 26624); SB;
        MFMA(accA[0][1], ba_13_1_1, aa_13_1_0); SB;
        const v4u ba_13_0_4 = LDS128(lin[2], 22528); SB;
        MFMA(accA[1][1], ba_13_0_1, aa_13_0_1); SB;
        MFMA(accA[1][1], ba_13_1_1, aa_13_1_1); SB;
        const v4u ba_13_1_4 = LDS128(lin[2], 27648); SB;
        WAITL(4); SB;
        MFMA(accA[0][2], ba_13_0_2, aa_13_0_0); SB;
        BAR(); SB;
        const v4u aa_14_0_0 = LDS128(swz0[0], 0); SB;
        MFMA(accA[0][2], ba_13_1_2, aa_13_1_0); SB;
        MFMA(accA[1][2], ba_13_0_2, aa_13_0_1); SB;
        const v4u aa_14_1_0 = LDS128(swz1[0], 0); SB;
        MFMA(accA[1][2], ba_13_1_2, aa_13_1_1); SB;
        const v4u ba_14_0_0 = LDS128(lin[0], 16384); SB;
        WAITL(5); SB;
        MFMA(accA[0][3], ba_13_0_3, aa_13_0_0); SB;
        const v4u ba_14_1_0 = LDS128(lin[0], 21504); SB;
        MFMA(accA[0][3], ba_13_1_3, aa_13_1_0); SB;
        MFMA(accA[1][3], ba_13_0_3, aa_13_0_1); SB;
        const v4u aa_14_0_1 = LDS128(swz0[0], 2048); SB;
        MFMA(accA[1][3], ba_13_1_3, aa_13_1_1); SB;
        const v4u aa_14_1_1 = LDS128(swz1[0], 2048); SB;
        WAITL(6); SB;
        MFMA(accA[0][4], ba_13_0_4, aa_13_0_0); SB;
        MFMA(accA[0][4], ba_13_1_4, aa_13_1_0); SB;
        const v4u ba_14_0_1 = LDS128(lin[0], 17408); SB;
        MFMA(accA[1][4], ba_13_0_4, aa_13_0_1); SB;
        const v4u ba_14_1_1 = LDS128(lin[0], 22528); SB;
        MFMA(accA[1][4], ba_13_1_4, aa_13_1_1); SB;
        WAITL(2); SB;
        MFMA(accA[0][0], ba_14_0_0, aa_14_0_0); SB;
        const v4u ba_14_0_2 = LDS128(lin[0], 18432); SB;
        MFMA(accA[0][0], ba_14_1_0, aa_14_1_0); SB;
        const v4u ba_14_1_2 = LDS128(lin[0], 23552); SB;
        MFMA(accA[1][0], ba_14_0_0, aa_14_0_1); SB;
        const v4u ba_14_0_3 = LDS128(lin[0], 19456); SB;
        MFMA(accA[1][0], ba_14_1_0, aa_14_1_1); SB;
        WAITL(3); SB;
        MFMA(accA[0][1], ba_14_0_1, aa_14_0_0); SB;
        const v4u ba_14_1_3 = LDS128(lin[0], 24576); SB;
        MFMA(accA[0][1], ba_14_1_1, aa_14_1_0); SB;
        const v4u ba_14_0_4 = LDS128(lin[0], 20480); SB;
        MFMA(accA[1][1], ba_14_0_1, aa_14_0_1); SB;
        MFMA(accA[1][1], ba_14_1_1, aa_14_1_1); SB;
        const v4u ba_14_1_4 = LDS128(lin[0], 25600); SB;
        WAITL(4); SB;
        MFMA(accA[0][2], ba_14_0_2, aa_14_0_0); SB;
        BAR(); SB;
        C_ASK_FB();
        const v4u aa_15_0_0 = LDS128(swz0[0], 26624); SB;
        MFMA(accA[0][2], ba_14_1_2, aa_14_1_0); SB;
        MFMA(accA[1][2], ba_14_0_2, aa_14_0_1); SB;
        const v4u aa_15_1_0 = LDS128(swz1[0], 26624); SB;
        MFMA(accA[1][2], ba_14_1_2, aa_14_1_1); SB;
        const v4u ba_15_0_0 = LDS128(lin[0], 43008); SB;
        WAITL(5); SB;
        MFMA(accA[0][3], ba_14_0_3, aa_14_0_0); SB;
        const v4u ba_15_1_0 = LDS128(lin[0], 48128); SB;
        MFMA(accA[0][3], ba_14_1_3, aa_14_1_0); SB;
        MFMA(accA[1][3], ba_14_0_3, aa_14_0_1); SB;
        const v4u aa_15_0_1 = LDS128(swz0[0], 28672); SB;
        MFMA(accA[1][3], ba_14_1_3, aa_14_1_1); SB;
        const v4u aa_15_1_1 = LDS128(swz1[0], 28672); SB;
        WAITL(6); SB;
        MFMA(accA[0][4], ba_14_0_4, aa_14_0_0); SB;
        MFMA(accA[0][4], ba_14_1_4, aa_14_1_0); SB;
        const v4u ba_15_0_1 = LDS128(lin[0], 44032); SB;
        MFMA(accA[1][4], ba_14_0_4, aa_14_0_1); SB;
        const v4u ba_15_1_1 = LDS128(lin[0], 49152); SB;
        MFMA(accA[1][4], ba_14_1_4, aa_14_1_1); SB;
        WAITL(2); SB;
        MFMA(accA[0][0], ba_15_0_0, aa_15_0_0); SB;
        const v4u ba_15_0_2 = LDS128(lin[0], 45056); SB;
        MFMA(accA[0][0], ba_15_1_0, aa_15_1_0); SB;
        const v4u ba_15_1_2 = LDS128(lin[0], 50176); SB;
        MFMA(accA[1][0], ba_15_0_0, aa_15_0_1); SB;
        const v4u ba_15_0_3 = LDS128(lin[0], 46080); SB;
        MFMA(accA[1][0], ba_15_1_0, aa_15_1_1); SB;
        WAITL(3); SB;
        MFMA(accA[0][1], ba_15_0_1, aa_15_0_0); SB;
        const v4u ba_15_1_3 = LDS128(lin[0], 51200); SB;
        MFMA(accA[0][1], ba_15_1_1, aa_15_1_0); SB;
        const v4u ba_15_0_4 = LDS128(lin[0], 47104); SB;
        MFMA(accA[1][1], ba_15_0_1, aa_15_0_1); SB;
        MFMA(accA[1][1], ba_15_1_1, aa_15_1_1); SB;
        const v4u ba_15_1_4 = LDS128(lin[0], 52224); SB;
        WAITL(4); SB;
        MFMA(accA[0][2], ba_15_0_2, aa_15_0_0); SB;
        BAR(); SB;
        const v4u aa_16_0_0 = LDS128(swz0[0], 53248); SB;
        MFMA(accA[0][2], ba_15_1_2, aa_15_1_0); SB;
        MFMA(accA[1][2], ba_15_0_2, aa_15_0_1); SB;
        const v4u aa_16_1_0 = LDS128(swz1[0], 53248); SB;
        MFMA(accA[1][2], ba_15_1_2, aa_15_1_1); SB;
        const v4u ba_16_0_0 = LDS128(lin[1], 4096); SB;
        WAITL(5); SB;
        MFMA(accA[0][3], ba_15_0_3, aa_15_0_0); SB;
        const v4u ba_16_1_0 = LDS128(lin[1], 9216); SB;
        MFMA(accA[0][3], ba_15_1_3, aa_15_1_0); SB;
        MFMA(accA[1][3], ba_15_0_3, aa_15_0_1); SB;
        const v4u aa_16_0_1 = LDS128(swz0[0], 55296); SB;
        MFMA(accA[1][3], ba_15_1_3, aa_15_1_1); SB;
        const v4u aa_16_1_1 = LDS128(swz1[0], 55296); SB;
        WAITL(6); SB;
        MFMA(accA[0][4], ba_15_0_4, aa_15_0_0); SB;
        MFMA(accA[0][4], ba_15_1_4, aa_15_1_0); SB;
        const v4u ba_16_0_1 = LDS128(lin[1], 5120); SB;
        MFMA(accA[1][4], ba_15_0_4, aa_15_0_1); SB;
        const v4u ba_16_1_1 = LDS128(lin[1], 10240); SB;
        MFMA(accA[1][4], ba_15_1_4, aa_15_1_1); SB;
        WAITL(2); SB;
        MFMA(accA[0][0], ba_16_0_0, aa_16_0_0); SB;
        const v4u ba_16_0_2 = LDS128(lin[1], 6144); SB;
        MFMA(accA[0][0], ba_16_1_0, aa_16_1_0); SB;
        const v4u ba_16_1_2 = LDS128(lin[1], 11264); SB;
        MFMA(accA[1][0], ba_16_0_0, aa_16_0_1); SB;
        const v4u ba_16_0_3 = LDS128(lin[1], 7168); SB;
        MFMA(accA[1][0], ba_16_1_0, aa_16_1_1); SB;
        WAITL(3); SB;
        MFMA(accA[0][1], ba_16_0_1, aa_16_0_0); SB;
        const v4u ba_16_1_3 = LDS128(lin[1], 12288); SB;
        MFMA(accA[0][1], ba_16_1_1, aa_16_1_0); SB;
        const v4u ba_16_0_4 = LDS128(lin[1], 8192); SB;
        MFMA(accA[1][1], ba_16_0_1, aa_16_0_1); SB;
        MFMA(accA[1][1], ba_16_1_1, aa_16_1_1); SB;
        const v4u ba_16_1_4 = LDS128(lin[1], 13312); SB;
        WAITL(4); SB;
        MFMA(accA[0][2], ba_16_0_2, aa_16_0_0); SB;
        BAR(); SB;
        const v4u aa_17_0_0 = LDS128(swz0[1], 14336); SB;
        MFMA(accA[0][2], ba_16_1_2, aa_16_1_0); SB;
        MFMA(accA[1][2], ba_16_0_2, aa_16_0_1); SB;
        const v4u aa_17_1_0 = LDS128(swz1[1], 14336); SB;
        MFMA(accA[1][2], ba_16_1_2, aa_16_1_1); SB;
        const v4u ba_17_0_0 = LDS128(lin[1], 30720); SB;
        WAITL(5); SB;
        MFMA(accA[0][3], ba_16_0_3, aa_16_0_0); SB;
        const v4u ba_17_1_0 = LDS128(lin[1], 35840); SB;
        MFMA(accA[0][3], ba_16_1_3, aa_16_1_0); SB;
        MFMA(accA[1][3], ba_16_0_3, aa_16_0_1); SB;
        const v4u aa_17_0_1 = LDS128(swz0[1], 16384); SB;
        MFMA(accA[1][3], ba_16_1_3, aa_16_1_1); SB;
        const v4u aa_17_1_1 = LDS128(swz1[1], 16384); SB;
        WAITL(6); SB;
        MFMA(accA[0][4], ba_16_0_4, aa_16_0_0); SB;
        MFMA(accA[0][4], ba_16_1_4, aa_16_1_0); SB;
        const v4u ba_17_0_1 = LDS128(lin[1], 31744); SB;
        MFMA(accA[1][4], ba_16_0_4, aa_16_0_1); SB;
        const v4u ba_17_1_1 = LDS128(lin[1], 36864); SB;
        MFMA(accA[1][4], ba_16_1_4, aa_16_1_1); SB;
        WAITL(2); SB;
        MFMA(accA[0][0], ba_17_0_0, aa_17_0_0); SB;
        const v4u ba_17_0_2 = LDS128(lin[1], 32768); SB;
        MFMA(accA[0][0], ba_17_1_0, aa_17_1_0); SB;
        const v4u ba_17_1_2 = LDS128(lin[1], 37888); SB;
        MFMA(accA[1][0], ba_17_0_0, aa_17_0_1); SB;
        const v4u ba_17_0_3 = LDS128(lin[1], 33792); SB;
        MFMA(accA[1][0], ba_17_1_0, aa_17_1_1); SB;
        WAITL(3); SB;
        MFMA(accA[0][1], ba_17_0_1, aa_17_0_0); SB;
        const v4u ba_17_1_3 = LDS128(lin[1], 38912); SB;
        MFMA(accA[0][1], ba_17_1_1, aa_17_1_0); SB;
        const v4u ba_17_0_4 = LDS128(lin[1], 34816); SB;
        MFMA(accA[1][1], ba_17_0_1, aa_17_0_1); SB;
        MFMA(accA[1][1], ba_17_1_1, aa_17_1_1); SB;
        const v4u ba_17_1_4 = LDS128(lin[1], 39936); SB;
        WAITL(4); SB;
        MFMA(accA[0][2], ba_17_0_2, aa_17_0_0); SB;
        BAR(); SB;
        const v4u aa_18_0_0 = LDS128(swz0[1], 40960); SB;
        MFMA(accA[0][2], ba_17_1_2, aa_17_1_0); SB;
        MFMA(accA[1][2], ba_17_0_2, aa_17_0_1); SB;
        const v4u aa_18_1_0 = LDS128(swz1[1], 40960); SB;
        MFMA(accA[1][2], ba_17_1_2, aa_17_1_1); SB;
        const v4u ba_18_0_0 = LDS128(lin[1], 57344); SB;
        WAITL(5); SB;
        MFMA(accA[0][3], ba_17_0_3, aa_17_0_0); SB;
        const v4u ba_18_1_0 = LDS128(lin[1], 62464); SB;
        MFMA(accA[0][3], ba_17_1_3, aa_17_1_0); SB;
        MFMA(accA[1][3], ba_17_0_3, aa_17_0_1); SB;
        const v4u aa_18_0_1 = LDS128(swz0[1], 43008); SB;
        MFMA(accA[1][3], ba_17_1_3, aa_17_1_1); SB;
        const v4u aa_18_1_1 = LDS128(swz1[1], 43008); SB;
        WAITL(6); SB;
        MFMA(accA[0][4], ba_17_0_4, aa_17_0_0); SB;
        MFMA(accA[0][4], ba_17_1_4, aa_17_1_0); SB;
        const v4u ba_18_0_1 = LDS128(lin[1], 58368); SB;
        MFMA(accA[1][4], ba_17_0_4, aa_17_0_1); SB;
        const v4u ba_18_1_1 = LDS128(lin[1], 63488); SB;
        MFMA(accA[1][4], ba_17_1_4, aa_17_1_1); SB;
        WAITL(2); SB;
        MFMA(accA[0][0], ba_18_0_0, aa_18_0_0); SB;
        const v4u ba_18_0_2 = LDS128(lin[1], 59392); SB;
        MFMA(accA[0][0], ba_18_1_0, aa_18_1_0); SB;
        const v4u ba_18_1_2 = LDS128(lin[1], 64512); SB;
        MFMA(accA[1][0], ba_18_0_0, aa_18_0_1); SB;
        const v4u ba_18_0_3 = LDS128(lin[1], 60416); SB;
        MFMA(accA[1][0], ba_18_1_0, aa_18_1_1); SB;
        WAITL(3); SB;
        MFMA(accA[0][1], ba_18_0_1, aa_18_0_0); SB;
        const v4u ba_18_1_3 = LDS128(lin[2], 0); SB;
        MFMA(accA[0][1], ba_18_1_1, aa_18_1_0); SB;
        const v4u ba_18_0_4 = LDS128(lin[1], 61440); SB;
        MFMA(accA[1][1], ba_18_0_1, aa_18_0_1); SB;
        MFMA(accA[1][1], ba_18_1_1, aa_18_1_1); SB;
        const v4u ba_18_1_4 = LDS128(lin[2], 1024); SB;
        WAITL(4); SB;
        MFMA(accA[0][2], ba_18_0_2, aa_18_0_0); SB;
        BAR(); SB;
        const v4u aa_19_0_0 = LDS128(swz0[2], 2048); SB;
        MFMA(accA[0][2], ba_18_1_2, aa_18_1_0); SB;
        MFMA(accA[1][2], ba_18_0_2, aa_18_0_1); SB;
        const v4u aa_19_1_0 = LDS128(swz1[2], 2048); SB;
        MFMA(accA[1][2], ba_18_1_2, aa_18_1_1); SB;
        const v4u ba_19_0_0 = LDS128(lin[2], 18432); SB;
        WAITL(5); SB;
        MFMA(accA[0][3], ba_18_0_3, aa_18_0_0); SB;
        const v4u ba_19_1_0 = LDS128(lin[2], 23552); SB;
        MFMA(accA[0][3], ba_18_1_3, aa_18_1_0); SB;
        MFMA(accA[1][3], ba_18_0_3, aa_18_0_1); SB;
        const v4u aa_19_0_1 = LDS128(swz0[2], 4096); SB;
        MFMA(accA[1][3], ba_18_1_3, aa_18_1_1); SB;
        const v4u aa_19_1_1 = LDS128(swz1[2], 4096); SB;
        WAITL(6); SB;
        MFMA(accA[0][4], ba_18_0_4, aa_18_0_0); SB;
        MFMA(accA[0][4], ba_18_1_4, aa_18_1_0); SB;
        const v4u ba_19_0_1 = LDS128(lin[2], 19456); SB;
        MFMA(accA[1][4], ba_18_0_4, aa_18_0_1); SB;
        const v4u ba_19_1_1 = LDS128(lin[2], 24576); SB;
        MFMA(accA[1][4], ba_18_1_4, aa_18_1_1); SB;
        WAITL(2); SB;
        MFMA(accA[0][0], ba_19_0_0, aa_19_0_0); SB;
        const v4u ba_19_0_2 = LDS128(lin[2], 20480); SB;
        MFMA(accA[0][0], ba_19_1_0, aa_19_1_0); SB;
        const v4u ba_19_1_2 = LDS128(lin[2], 25600); SB;
        MFMA(accA[1][0], ba_19_0_0, aa_19_0_1); SB;
        const v4u ba_19_0_3 = LDS128(lin[2], 21504); SB;
        MFMA(accA[1][0], ba_19_1_0, aa_19_1_1); SB;
        WAITL(3); SB;
        MFMA(accA[0][1], ba_19_0_1, aa_19_0_0); SB;
        const v4u ba_19_1_3 = LDS128(lin[2], 26624); SB;
        MFMA(accA[0][1], ba_19_1_1, aa_19_1_0); SB;
        const v4u ba_19_0_4 = LDS128(lin[2], 22528); SB;
        MFMA(accA[1][1], ba_19_0_1, aa_19_0_1); SB;
        MFMA(accA[1][1], ba_19_1_1, aa_19_1_1); SB;
        const v4u ba_19_1_4 = LDS128(lin[2], 27648); SB;
        WAITL(4); SB;
        MFMA(accA[0][2], ba_19_0_2, aa_19_0_0); SB;
        BAR(); SB;
        const v4u aa_20_0_0 = LDS128(swz0[0], 0); SB;
        MFMA(accA[0][2], ba_19_1_2, aa_19_1_0); SB;
        MFMA(accA[1][2], ba_19_0_2, aa_19_0_1); SB;
        const v4u aa_20_1_0 = LDS128(swz1[0], 0); SB;
        MFMA(accA[1][2], ba_19_1_2, aa_19_1_1); SB;
        const v4u ba_20_0_0 = LDS128(lin[0], 16384); SB;
        WAITL(5); SB;
        MFMA(accA[0][3], ba_19_0_3, aa_19_0_0); SB;
        const v4u ba_20_1_0 = LDS128(lin[0], 21504); SB;
        MFMA(accA[0][3], ba_19_1_3, aa_19_1_0); SB;
        MFMA(accA[1][3], ba_19_0_3, aa_19_0_1); SB;
        const v4u aa_20_0_1 = LDS128(swz0[0], 2048); SB;
        MFMA(accA[1][3], ba_19_1_3, aa_19_1_1); SB;
        const v4u aa_20_1_1 = LDS128(swz1[0], 2048); SB;
        WAITL(6); SB;
        MFMA(accA[0][4], ba_19_0_4, aa_19_0_0); SB;
        MFMA(accA[0][4], ba_19_1_4, aa_19_1_0); SB;
        const v4u ba_20_0_1 = LDS128(lin[0], 17408); SB;
        MFMA(accA[1][4], ba_19_0_4, aa_19_0_1); SB;
        const v4u ba_20_1_1 = LDS128(lin[0], 22528); SB;
        MFMA(accA[1][4], ba_19_1_4, aa_19_1_1); SB;
        WAITL(2); SB;
        MFMA(accA[0][0], ba_20_0_0, aa_20_0_0); SB;
        const v4u ba_20_0_2 = LDS128(lin[0], 18432); SB;
        MFMA(accA[0][0], ba_20_1_0, aa_20_1_0); SB;
        const v4u ba_20_1_2 = LDS128(lin[0], 23552); SB;
        MFMA(accA[1][0], ba_20_0_0, aa_20_0_1); SB;
        const v4u ba_20_0_3 = LDS128(lin[0], 19456); SB;
        MFMA(accA[1][0], ba_20_1_0, aa_20_1_1); SB;
        WAITL(3); SB;
        MFMA(accA[0][1], ba_20_0_1, aa_20_0_0); SB;
        const v4u ba_20_1_3 = LDS128(lin[0], 24576); SB;
        MFMA(accA[0][1], ba_20_1_1, aa_20_1_0); SB;
        const v4u ba_20_0_4 = LDS128(lin[0], 20480); SB;
        MFMA(accA[1][1], ba_20_0_1, aa_20_0_1); SB;
        MFMA(accA[1][1], ba_20_1_1, aa_20_1_1); SB;
        const v4u ba_20_1_4 = LDS128(lin[0], 25600); SB;
        WAITL(4); SB;
        MFMA(accA[0][2], ba_20_0_2, aa_20_0_0); SB;
        BAR(); SB;
        C_CHK_FB();
        const v4u aa_21_0_0 = LDS128(swz0[0], 26624); SB;
        MFMA(accA[0][2], ba_20_1_2, aa_20_1_0); SB;
        MFMA(accA[1][2], ba_20_0_2, aa_20_0_1); SB;
        const v4u aa_21_1_0 = LDS128(swz1[0], 26624); SB;
        MFMA(accA[1][2], ba_20_1_2, aa_20_1_1); SB;
        const v4u ba_21_0_0 = LDS128(lin[0], 43008); SB;
        WAITL(5); SB;
        MFMA(accA[0][3], ba_20_0_3, aa_20_0_0); SB;
        const v4u ba_21_1_0 = LDS128(lin[0], 48128); SB;
        MFMA(accA[0][3], ba_20_1_3, aa_20_1_0); SB;
        MFMA(accA[1][3], ba_20_0_3, aa_20_0_1); SB;
        const v4u aa_21_0_1 = LDS128(swz0[0], 28672); SB;
        MFMA(accA[1][3], ba_20_1_3, aa_20_1_1); SB;
        const v4u aa_21_1_1 = LDS128(swz1[0], 28672); SB;
        WAITL(6); SB;
        MFMA(accA[0][4], ba_20_0_4, aa_20_0_0); SB;
        MFMA(accA[0][4], ba_20_1_4, aa_20_1_0); SB;
        const v4u ba_21_0_1 = LDS128(lin[0], 44032); SB;
        MFMA(accA[1][4], ba_20_0_4, aa_20_0_1); SB;
        const v4u ba_21_1_1 = LDS128(lin[0], 49152); SB;
        MFMA(accA[1][4], ba_20_1_4, aa_20_1_1); SB;
        WAITL(2); SB;
        MFMA(accA[0][0], ba_21_0_0, aa_21_0_0); SB;
        const v4u ba_21_0_2 = LDS128(lin[0], 45056); SB;
        MFMA(accA[0][0], ba_21_1_0, aa_21_1_0); SB;
        const v4u ba_21_1_2 = LDS128(lin[0], 50176); SB;
        MFMA(accA[1][0], ba_21_0_0, aa_21_0_1); SB;
        const v4u ba_21_0_3 = LDS128(lin[0], 46080); SB;
        MFMA(accA[1][0], ba_21_1_0, aa_21_1_1); SB;
        WAITL(3); SB;
        MFMA(accA[0][1], ba_21_0_1, aa_21_0_0); SB;
        const v4u ba_21_1_3 = LDS128(lin[0], 51200); SB;
        MFMA(accA[0][1], ba_21_1_1, aa_21_1_0); SB;
        const v4u ba_21_0_4 = LDS128(lin[0], 47104); SB;
        MFMA(accA[1][1], ba_21_0_1, aa_21_0_1); SB;
        MFMA(accA[1][1], ba_21_1_1, aa_21_1_1); SB;
        const v4u ba_21_1_4 = LDS128(lin[0], 52224); SB;
        WAITL(4); SB;
        MFMA(accA[0][2], ba_21_0_2, aa_21_0_0); SB;
        BAR(); SB;
        const v4u aa_22_0_0 = LDS128(swz0[0], 53248); SB;
        MFMA(accA[0][2], ba_21_1_2, aa_21_1_0); SB;
        MFMA(accA[1][2], ba_21_0_2, aa_21_0_1); SB;
        const v4u aa_22_1_0 = LDS128(swz1[0], 53248); SB;
        MFMA(accA[1][2], ba_21_1_2, aa_21_1_1); SB;
        const v4u ba_22_0_0 = LDS128(lin[1], 4096); SB;
        WAITL(5); SB;
        MFMA(accA[0][3], ba_21_0_3, aa_21_0_0); SB;
        const v4u ba_22_1_0 = LDS128(lin[1], 9216); SB;
        MFMA(accA[0][3], ba_21_1_3, aa_21_1_0); SB;
        MFMA(accA[1][3], ba_21_0_3, aa_21_0_1); SB;
        const v4u aa_22_0_1 = LDS128(swz0[0], 55296); SB;
        MFMA(accA[1][3], ba_21_1_3, aa_21_1_1); SB;
        const v4u aa_22_1_1 = LDS128(swz1[0], 55296); SB;
        WAITL(6); SB;
        MFMA(accA[0][4], ba_21_0_4, aa_21_0_0); SB;
        MFMA(accA[0][4], ba_21_1_4, aa_21_1_0); SB;
        const v4u ba_22_0_1 = LDS128(lin[1], 5120); SB;
        MFMA(accA[1][4], ba_21_0_4, aa_21_0_1); SB;
        const v4u ba_22_1_1 = LDS128(lin[1], 10240); SB;
        MFMA(accA[1][4], ba_21_1_4, aa_21_1_1); SB;
        WAITL(2); SB;
        MFMA(accA[0][0], ba_22_0_0, aa_22_0_0); SB;
        const v4u ba_22_0_2 = LDS128(lin[1], 6144); SB;
        MFMA(accA[0][0], ba_22_1_0, aa_22_1_0); SB;
        const v4u ba_22_1_2 = LDS128(lin[1], 11264); SB;
        MFMA(accA[1][0], ba_22_0_0, aa_22_0_1); SB;
        const v4u ba_22_0_3 = LDS128(lin[1], 7168); SB;
        MFMA(accA[1][0], ba_22_1_0, aa_22_1_1); SB;
        WAITL(3); SB;
        MFMA(accA[0][1], ba_22_0_1, aa_22_0_0); SB;
        const v4u ba_22_1_3 = LDS128(lin[1], 12288); SB;
        MFMA(accA[0][1], ba_22_1_1, aa_22_1_0); SB;
        const v4u ba_22_0_4 = LDS128(lin[1], 8192); SB;
        MFMA(accA[1][1], ba_22_0_1, aa_22_0_1); SB;
        MFMA(accA[1][1], ba_22_1_1, aa_22_1_1); SB;
        const v4u ba_22_1_4 = LDS128(lin[1], 13312); SB;
        WAITL(4); SB;
        MFMA(accA[0][2], ba_22_0_2, aa_22_0_0); SB;
        BAR(); SB;
        const v4u aa_23_0_0 = LDS128(swz0[1], 14336); SB;
        MFMA(accA[0][2], ba_22_1_2, aa_22_1_0); SB;
        MFMA(accA[1][2], ba_22_0_2, aa_22_0_1); SB;
        const v4u aa_23_1_0 = LDS128(swz1[1], 14336); SB;
        MFMA(accA[1][2], ba_22_1_2, aa_22_1_1); SB;
        const v4u ba_23_0_0 = LDS128(lin[1], 30720); SB;
        WAITL(5); SB;
        MFMA(accA[0][3], ba_22_0_3, aa_22_0_0); SB;
        const v4u ba_23_1_0 = LDS128(lin[1], 35840); SB;
        MFMA(accA[0][3], ba_22_1_3, aa_22_1_0); SB;
        MFMA(accA[1][3], ba_22_0_3, aa_22_0_1); SB;
        const v4u aa_23_0_1 = LDS128(swz0[1], 16384); SB;
        MFMA(accA[1][3], ba_22_1_3, aa_22_1_1); SB;
        const v4u aa_23_1_1 = LDS128(swz1[1], 16384); SB;
        WAITL(6); SB;
        MFMA(accA[0][4], ba_22_0_4, aa_22_0_0); SB;
        MFMA(accA[0][4], ba_22_1_4, aa_22_1_0); SB;
        const v4u ba_23_0_1 = LDS128(lin[1], 31744); SB;
        MFMA(accA[1][4], ba_22_0_4, aa_22_0_1); SB;
        const v4u ba_23_1_1 = LDS128(lin[1], 36864); SB;
        MFMA(accA[1][4], ba_22_1_4, aa_22_1_1); SB;
        WAITL(2); SB;
        MFMA(accA[0][0], ba_23_0_0, aa_23_0_0); SB;
        const v4u ba_23_0_2 = LDS128(lin[1], 32768); SB;
        MFMA(accA[0][0], ba_23_1_0, aa_23_1_0); SB;
        const v4u ba_23_1_2 = LDS128(lin[1], 37888); SB;
        MFMA(accA[1][0], ba_23_0_0, aa_23_0_1); SB;
        const v4u ba_23_0_3 = LDS128(lin[1], 33792); SB;
        MFMA(accA[1][0], ba_23_1_0, aa_23_1_1); SB;
        WAITL(3); SB;
        MFMA(accA[0][1], ba_23_0_1, aa_23_0_0); SB;
        const v4u ba_23_1_3 = LDS128(lin[1], 38912); SB;
        MFMA(accA[0][1], ba_23_1_1, aa_23_1_0); SB;
        const v4u ba_23_0_4 = LDS128(lin[1], 34816); SB;
        MFMA(accA[1][1], ba_23_0_1, aa_23_0_1); SB;
        MFMA(accA[1][1], ba_23_1_1, aa_23_1_1); SB;
        const v4u ba_23_1_4 = LDS128(lin[1], 39936); SB;
        WAITL(4); SB;
        MFMA(accA[0][2], ba_23_0_2, aa_23_0_0); SB;
        BAR(); SB;
        const v4u aa_24_0_0 = LDS128(swz0[1], 40960); SB;
        MFMA(accA[0][2], ba_23_1_2, aa_23_1_0); SB;
        MFMA(accA[1][2], ba_23_0_2, aa_23_0_1); SB;
        const v4u aa_24_1_0 = LDS128(swz1[1], 40960); SB;
        MFMA(accA[1][2], ba_23_1_2, aa_23_1_1); SB;
        const v4u ba_24_0_0 = LDS128(lin[1], 57344); SB;
        WAITL(5); SB;
        MFMA(accA[0][3], ba_23_0_3, aa_23_0_0); SB;
        const v4u ba_24_1_0 = LDS128(lin[1], 62464); SB;
        MFMA(accA[0][3], ba_23_1_3, aa_23_1_0); SB;
        MFMA(accA[1][3], ba_23_0_3, aa_23_0_1); SB;
        const v4u aa_24_0_1 = LDS128(swz0[1], 43008); SB;
        MFMA(accA[1][3], ba_23_1_3, aa_23_1_1); SB;
        const v4u aa_24_1_1 = LDS128(swz1[1], 43008); SB;
        WAITL(6); SB;
        MFMA(accA[0][4], ba_23_0_4, aa_23_0_0); SB;
        MFMA(accA[0][4], ba_23_1_4, aa_23_1_0); SB;
        const v4u ba_24_0_1 = LDS128(lin[1], 58368); SB;
        MFMA(accA[1][4], ba_23_0_4, aa_23_0_1); SB;
        const v4u ba_24_1_1 = LDS128(lin[1], 63488); SB;
        MFMA(accA[1][4], ba_23_1_4, aa_23_1_1); SB;
        WAITL(2); SB;
        MFMA(accA[0][0], ba_24_0_0, aa_24_0_0); SB;
        const v4u ba_24_0_2 = LDS128(lin[1], 59392); SB;
        MFMA(accA[0][0], ba_24_1_0, aa_24_1_0); SB;
        const v4u ba_24_1_2 = LDS128(lin[1], 64512); SB;
        MFMA(accA[1][0], ba_24_0_0, aa_24_0_1); SB;
        const v4u ba_24_0_3 = LDS128(lin[1], 60416); SB;
        MFMA(accA[1][0], ba_24_1_0, aa_24_1_1); SB;
        WAITL(3); SB;
        MFMA(accA[0][1], ba_24_0_1, aa_24_0_0); SB;
        const v4u ba_24_1_3 = LDS128(lin[2], 0); SB;
        MFMA(accA[0][1], ba_24_1_1, aa_24_1_0); SB;
        const v4u ba_24_0_4 = LDS128(lin[1], 61440); SB;
        MFMA(accA[1][1], ba_24_0_1, aa_24_0_1); SB;
        MFMA(accA[1][1], ba_24_1_1, aa_24_1_1); SB;
        const v4u ba_24_1_4 = LDS128(lin[2], 1024); SB;
        WAITL(4); SB;
        MFMA(accA[0][2], ba_24_0_2, aa_24_0_0); SB;
        BAR(); SB;
        const v4u aa_25_0_0 = LDS128(swz0[2], 2048); SB;
        MFMA(accA[0][2], ba_24_1_2, aa_24_1_0); SB;
        MFMA(accA[1][2], ba_24_0_2, aa_24_0_1); SB;
        const v4u aa_25_1_0 = LDS128(swz1[2], 2048); SB;
        MFMA(accA[1][2], ba_24_1_2, aa_24_1_1); SB;
        const v4u ba_25_0_0 = LDS128(lin[2], 18432); SB;
        WAITL(5); SB;
        MFMA(accA[0][3], ba_24_0_3, aa_24_0_0); SB;
        const v4u ba_25_1_0 = LDS128(lin[2], 23552); SB;
        MFMA(accA[0][3], ba_24_1_3, aa_24_1_0); SB;
        MFMA(accA[1][3], ba_24_0_3, aa_24_0_1); SB;
        const v4u aa_25_0_1 = LDS128(swz0[2], 4096); SB;
        MFMA(accA[1][3], ba_24_1_3, aa_24_1_1); SB;
        const v4u aa_25_1_1 = LDS128(swz1[2], 4096); SB;
        WAITL(6); SB;
        MFMA(accA[0][4], ba_24_0_4, aa_24_0_0); SB;
        MFMA(accA[0][4], ba_24_1_4, aa_24_1_0); SB;
        const v4u ba_25_0_1 = LDS128(lin[2], 19456); SB;
        MFMA(accA[1][4], ba_24_0_4, aa_24_0_1); SB;
        const v4u ba_25_1_1 = LDS128(lin[2], 24576); SB;
        MFMA(accA[1][4], ba_24_1_4, aa_24_1_1); SB;
        WAITL(2); SB;
        MFMA(accA[0][0], ba_25_0_0, aa_25_0_0); SB;
        const v4u ba_25_0_2 = LDS128(lin[2], 20480); SB;
        MFMA(accA[0][0], ba_25_1_0, aa_25_1_0); SB;
        const v4u ba_25_1_2 = LDS128(lin[2], 25600); SB;
        MFMA(accA[1][0], ba_25_0_0, aa_25_0_1); SB;
        const v4u ba_25_0_3 = LDS128(lin[2], 21504); SB;
        MFMA(accA[1][0], ba_25_1_0, aa_25_1_1); SB;
        WAITL(3); SB;
        MFMA(accA[0][1], ba_25_0_1, aa_25_0_0); SB;
        const v4u ba_25_1_3 = LDS128(lin[2], 26624); SB;
        MFMA(accA[0][1], ba_25_1_1, aa_25_1_0); SB;
        const v4u ba_25_0_4 = LDS128(lin[2], 22528); SB;
        MFMA(accA[1][1], ba_25_0_1, aa_25_0_1); SB;
        MFMA(accA[1][1], ba_25_1_1, aa_25_1_1); SB;
        const v4u ba_25_1_4 = LDS128(lin[2], 27648); SB;
        WAITL(4); SB;
        MFMA(accA[0][2], ba_25_0_2, aa_25_0_0); SB;
        BAR(); SB;
        const v4u aa_26_0_0 = LDS128(swz0[0], 0); SB;
        MFMA(accA[0][2], ba_25_1_2, aa_25_1_0); SB;
        MFMA(accA[1][2], ba_25_0_2, aa_25_0_1); SB;
        const v4u aa_26_1_0 = LDS128(swz1[0], 0); SB;
        MFMA(accA[1][2], ba_25_1_2, aa_25_1_1); SB;
        const v4u ba_26_0_0 = LDS128(lin[0], 16384); SB;
        WAITL(5); SB;
        MFMA(accA[0][3], ba_25_0_3, aa_25_0_0); SB;
        const v4u ba_26_1_0 = LDS128(lin[0], 21504); SB;
        MFMA(accA[0][3], ba_25_1_3, aa_25_1_0); SB;
        MFMA(accA[1][3], ba_25_0_3, aa_25_0_1); SB;
        const v4u aa_26_0_1 = LDS128(swz0[0], 2048); SB;
        MFMA(accA[1][3], ba_25_1_3, aa_25_1_1); SB;
        const v4u aa_26_1_1 = LDS128(swz1[0], 2048); SB;
        WAITL(6); SB;
        MFMA(accA[0][4], ba_25_0_4, aa_25_0_0); SB;
        MFMA(accA[0][4], ba_25_1_4, aa_25_1_0); SB;
        const v4u ba_26_0_1 = LDS128(lin[0], 17408); SB;
        MFMA(accA[1][4], ba_25_0_4, aa_25_0_1); SB;
        const v4u ba_26_1_1 = LDS128(lin[0], 22528); SB;
        MFMA(accA[1][4], ba_25_1_4, aa_25_1_1); SB;
        WAITL(2); SB;
        MFMA(accA[0][0], ba_26_0_0, aa_26_0_0); SB;
        const v4u ba_26_0_2 = LDS128(lin[0], 18432); SB;
        MFMA(accA[0][0], ba_26_1_0, aa_26_1_0); SB;
        const v4u ba_26_1_2 = LDS128(lin[0], 23552); SB;
        MFMA(accA[1][0], ba_26_0_0, aa_26_0_1); SB;
        const v4u ba_26_0_3 = LDS128(lin[0], 19456); SB;
        MFMA(accA[1][0], ba_26_1_0, aa_26_1_1); SB;
        WAITL(3); SB;
        MFMA(accA[0][1], ba_26_0_1, aa_26_0_0); SB;
        const v4u ba_26_1_3 = LDS128(lin[0], 24576); SB;
        MFMA(accA[0][1], ba_26_1_1, aa_26_1_0); SB;
        const v4u ba_26_0_4 = LDS128(lin[0], 20480); SB;
        MFMA(accA[1][1], ba_26_0_1, aa_26_0_1); SB;
        MFMA(accA[1][1], ba_26_1_1, aa_26_1_1); SB;
        const v4u ba_26_1_4 = LDS128(lin[0], 25600); SB;
        WAITL(4); SB;
        MFMA(accA[0][2], ba_26_0_2, aa_26_0_0); SB;
        BAR(); SB;
        PRE_A();
        const v4u aa_27_0_0 = LDS128(swz0[0], 26624); SB;
        MFMA(accA[0][2], ba_26_1_2, aa_26_1_0); SB;
        MFMA(accA[1][2], ba_26_0_2, aa_26_0_1); SB;
        const v4u aa_27_1_0 = LDS128(swz1[0], 26624); SB;
        MFMA(accA[1][2], ba_26_1_2, aa_26_1_1); SB;
        const v4u ba_27_0_0 = LDS128(lin[0], 43008); SB;
        WAITL(5); SB;
        MFMA(accA[0][3], ba_26_0_3, aa_26_0_0); SB;
        const v4u ba_27_1_0 = LDS128(lin[0], 48128); SB;
        MFMA(accA[0][3], ba_26_1_3, aa_26_1_0); SB;
        MFMA(accA[1][3], ba_26_0_3, aa_26_0_1); SB;
        const v4u aa_27_0_1 = LDS128(swz0[0], 28672); SB;
        MFMA(accA[1][3], ba_26_1_3, aa_26_1_1); SB;
        const v4u aa_27_1_1 = LDS128(swz1[0], 28672); SB;
        WAITL(6); SB;
        MFMA(accA[0][4], ba_26_0_4, aa_26_0_0); SB;
        MFMA(accA[0][4], ba_26_1_4, aa_26_1_0); SB;
        const v4u ba_27_0_1 = LDS128(lin[0], 44032); SB;
        MFMA(accA[1][4], ba_26_0_4, aa_26_0_1); SB;
        const v4u ba_27_1_1 = LDS128(lin[0], 49152); SB;
        MFMA(accA[1][4], ba_26_1_4, aa_26_1_1); SB;
        WAITL(2); SB;
        MFMA(accA[0][0], ba_27_0_0, aa_27_0_0); SB;
        const v4u ba_27_0_2 = LDS128(lin[0], 45056); SB;
        MFMA(accA[0][0], ba_27_1_0, aa_27_1_0); SB;
        const v4u ba_27_1_2 = LDS128(lin[0], 50176); SB;
        MFMA(accA[1][0], ba_27_0_0, aa_27_0_1); SB;
        const v4u ba_27_0_3 = LDS128(lin[0], 46080); SB;
        MFMA(accA[1][0], ba_27_1_0, aa_27_1_1); SB;
        WAITL(3); SB;
        MFMA(accA[0][1], ba_27_0_1, aa_27_0_0); SB;
        const v4u ba_27_1_3 = LDS128(lin[0], 51200); SB;
        MFMA(accA[0][1], ba_27_1_1, aa_27_1_0); SB;
        const v4u ba_27_0_4 = LDS128(lin[0], 47104); SB;
        MFMA(accA[1][1], ba_27_0_1, aa_27_0_1); SB;
        MFMA(accA[1][1], ba_27_1_1, aa_27_1_1); SB;
        const v4u ba_27_1_4 = LDS128(lin[0], 52224); SB;
        WAITL(4); SB;
        MFMA(accA[0][2], ba_27_0_2, aa_27_0_0); SB;
        BAR(); SB;
        const v4u aa_28_0_0 = LDS128(swz0[0], 53248); SB;
        MFMA(accA[0][2], ba_27_1_2, aa_27_1_0); SB;
        MFMA(accA[1][2], ba_27_0_2, aa_27_0_1); SB;
        const v4u aa_28_1_0 = LDS128(swz1[0], 53248); SB;
        MFMA(accA[1][2], ba_27_1_2, aa_27_1_1); SB;
        const v4u ba_28_0_0 = LDS128(lin[1], 4096); SB;
        WAITL(5); SB;
        MFMA(accA[0][3], ba_27_0_3, aa_27_0_0); SB;
        const v4u ba_28_1_0 = LDS128(lin[1], 9216); SB;
        MFMA(accA[0][3], ba_27_1_3, aa_27_1_0); SB;
        MFMA(accA[1][3], ba_27_0_3, aa_27_0_1); SB;
        const v4u aa_28_0_1 = LDS128(swz0[0], 55296); SB;
        MFMA(accA[1][3], ba_27_1_3, aa_27_1_1); SB;
        const v4u aa_28_1_1 = LDS128(swz1[0], 55296); SB;
        WAITL(6); SB;
        MFMA(accA[0][4], ba_27_0_4, aa_27_0_0); SB;
        MFMA(accA[0][4], ba_27_1_4, aa_27_1_0); SB;
        const v4u ba_28_0_1 = LDS128(lin[1], 5120); SB;
        MFMA(accA[1][4], ba_27_0_4, aa_27_0_1); SB;
        const v4u ba_28_1_1 = LDS128(lin[1], 10240); SB;
        MFMA(accA[1][4], ba_27_1_4, aa_27_1_1); SB;
        WAITL(2); SB;
        MFMA(accA[0][0], ba_28_0_0, aa_28_0_0); SB;
        const v4u ba_28_0_2 = LDS128(lin[1], 6144); SB;
        MFMA(accA[0][0], ba_28_1_0, aa_28_1_0); SB;
        const v4u ba_28_1_2 = LDS128(lin[1], 11264); SB;
        MFMA(accA[1][0], ba_28_0_0, aa_28_0_1); SB;
        const v4u ba_28_0_3 = LDS128(lin[1], 7168); SB;
        MFMA(accA[1][0], ba_28_1_0, aa_28_1_1); SB;
        WAITL(3); SB;
        MFMA(accA[0][1], ba_28_0_1, aa_28_0_0); SB;
        const v4u ba_28_1_3 = LDS128(lin[1], 12288); SB;
        MFMA(accA[0][1], ba_28_1_1, aa_28_1_0); SB;
        const v4u ba_28_0_4 = LDS128(lin[1], 8192); SB;
        MFMA(accA[1][1], ba_28_0_1, aa_28_0_1); SB;
        MFMA(accA[1][1], ba_28_1_1, aa_28_1_1); SB;
        const v4u ba_28_1_4 = LDS128(lin[1], 13312); SB;
        WAITL(4); SB;
        MFMA(accA[0][2], ba_28_0_2, aa_28_0_0); SB;
        BAR(); SB;
        const v4u aa_29_0_0 = LDS128(swz0[1], 14336); SB;
        MFMA(accA[0][2], ba_28_1_2, aa_28_1_0); SB;
        MFMA(accA[1][2], ba_28_0_2, aa_28_0_1); SB;
        const v4u aa_29_1_0 = LDS128(swz1[1], 14336); SB;
        MFMA(accA[1][2], ba_28_1_2, aa_28_1_1); SB;
        const v4u ba_29_0_0 = LDS128(lin[1], 30720); SB;
        WAITL(5); SB;
        MFMA(accA[0][3], ba_28_0_3, aa_28_0_0); SB;
        const v4u ba_29_1_0 = LDS128(lin[1], 35840); SB;
        MFMA(accA[0][3], ba_28_1_3, aa_28_1_0); SB;
        MFMA(accA[1][3], ba_28_0_3, aa_28_0_1); SB;
        const v4u aa_29_0_1 = LDS128(swz0[1], 16384); SB;
        MFMA(accA[1][3], ba_28_1_3, aa_28_1_1); SB;
        const v4u aa_29_1_1 = LDS128(swz1[1], 16384); SB;
        WAITL(6); SB;
        MFMA(accA[0][4], ba_28_0_4, aa_28_0_0); SB;
        MFMA(accA[0][4], ba_28_1_4, aa_28_1_0); SB;
        const v4u ba_29_0_1 = LDS128(lin[1], 31744); SB;
        MFMA(accA[1][4], ba_28_0_4, aa_28_0_1); SB;
        const v4u ba_29_1_1 = LDS128(lin[1], 36864); SB;
        MFMA(accA[1][4], ba_28_1_4, aa_28_1_1); SB;
        WAITL(2); SB;
        MFMA(accA[0][0], ba_29_0_0, aa_29_0_0); SB;
        const v4u ba_29_0_2 = LDS128(lin[1], 32768); SB;
        MFMA(accA[0][0], ba_29_1_0, aa_29_1_0); SB;
        const v4u ba_29_1_2 = LDS128(lin[1], 37888); SB;
        MFMA(accA[1][0], ba_29_0_0, aa_29_0_1); SB;
        const v4u ba_29_0_3 = LDS128(lin[1], 33792); SB;
        MFMA(accA[1][0], ba_29_1_0, aa_29_1_1); SB;
        WAITL(3); SB;
        MFMA(accA[0][1], ba_29_0_1, aa_29_0_0); SB;
        const v4u ba_29_1_3 = LDS128(lin[1], 38912); SB;
        MFMA(accA[0][1], ba_29_1_1, aa_29_1_0); SB;
        const v4u ba_29_0_4 = LDS128(lin[1], 34816); SB;
        MFMA(accA[1][1], ba_29_0_1, aa_29_0_1); SB;
        MFMA(accA[1][1], ba_29_1_1, aa_29_1_1); SB;
        const v4u ba_29_1_4 = LDS128(lin[1], 39936); SB;
        WAITL(4); SB;
        MFMA(accA[0][2], ba_29_0_2, aa_29_0_0); SB;
        BAR(); SB;
        const v4u aa_30_0_0 = LDS128(swz0[1], 40960); SB;
        MFMA(accA[0][2], ba_29_1_2, aa_29_1_0); SB;
        MFMA(accA[1][2], ba_29_0_2, aa_29_0_1); SB;
        const v4u aa_30_1_0 = LDS128(swz1[1], 40960); SB;
        MFMA(accA[1][2], ba_29_1_2, aa_29_1_1); SB;
        const v4u ba_30_0_0 = LDS128(lin[1], 57344); SB;
        WAITL(5); SB;
        MFMA(accA[0][3], ba_29_0_3, aa_29_0_0); SB;
        const v4u ba_30_1_0 = LDS128(lin[1], 62464); SB;
        MFMA(accA[0][3], ba_29_1_3, aa_29_1_0); SB;
        MFMA(accA[1][3], ba_29_0_3, aa_29_0_1); SB;
        const v4u aa_30_0_1 = LDS128(swz0[1], 43008); SB;
        MFMA(accA[1][3], ba_29_1_3, aa_29_1_1); SB;
        const v4u aa_30_1_1 = LDS128(swz1[1], 43008); SB;
        WAITL(6); SB;
        MFMA(accA[0][4], ba_29_0_4, aa_29_0_0); SB;
        MFMA(accA[0][4], ba_29_1_4, aa_29_1_0); SB;
        const v4u ba_30_0_1 = LDS128(lin[1], 58368); SB;
        MFMA(accA[1][4], ba_29_0_4, aa_29_0_1); SB;
        const v4u ba_30_1_1 = LDS128(lin[1], 63488); SB;
        MFMA(accA[1][4], ba_29_1_4, aa_29_1_1); SB;
        WAITL(2); SB;
        MFMA(accA[0][0], ba_30_0_0, aa_30_0_0); SB;
        const v4u ba_30_0_2 = LDS128(lin[1], 59392); SB;
        MFMA(accA[0][0], ba_30_1_0, aa_30_1_0); SB;
        const v4u ba_30_1_2 = LDS128(lin[1], 64512); SB;
        MFMA(accA[1][0], ba_30_0_0, aa_30_0_1); SB;
        const v4u ba_30_0_3 = LDS128(lin[1], 60416); SB;
        MFMA(accA[1][0], ba_30_1_0, aa_30_1_1); SB;
        WAITL(3); SB;
        MFMA(accA[0][1], ba_30_0_1, aa_30_0_0); SB;
        const v4u ba_30_1_3 = LDS128(lin[2], 0); SB;
        MFMA(accA[0][1], ba_30_1_1, aa_30_1_0); SB;
        const v4u ba_30_0_4 = LDS128(lin[1], 61440); SB;
        MFMA(accA[1][1], ba_30_0_1, aa_30_0_1); SB;
        MFMA(accA[1][1], ba_30_1_1, aa_30_1_1); SB;
        const v4u ba_30_1_4 = LDS128(lin[2], 1024); SB;
        WAITL(4); SB;
        MFMA(accA[0][2], ba_30_0_2, aa_30_0_0); SB;
        BAR(); SB;
        const v4u aa_31_0_0 = LDS128(swz0[2], 2048); SB;
        MFMA(accA[0][2], ba_30_1_2, aa_30_1_0); SB;
        MFMA(accA[1][2], ba_30_0_2, aa_30_0_1); SB;
        const v4u aa_31_1_0 = LDS128(swz1[2], 2048); SB;
        MFMA(accA[1][2], ba_30_1_2, aa_30_1_1); SB;
        const v4u ba_31_0_0 = LDS128(lin[2], 18432); SB;
        WAITL(5); SB;
        MFMA(accA[0][3], ba_30_0_3, aa_30_0_0); SB;
        const v4u ba_31_1_0 = LDS128(lin[2], 23552); SB;
        MFMA(accA[0][3], ba_30_1_3, aa_30_1_0); SB;
        MFMA(accA[1][3], ba_30_0_3, aa_30_0_1); SB;
        const v4u aa_31_0_1 = LDS128(swz0[2], 4096); SB;
        MFMA(accA[1][3], ba_30_1_3, aa_30_1_1); SB;
        const v4u aa_31_1_1 = LDS128(swz1[2], 4096); SB;
        WAITL(6); SB;
        MFMA(accA[0][4], ba_30_0_4, aa_30_0_0); SB;
        MFMA(accA[0][4], ba_30_1_4, aa_30_1_0); SB;
        const v4u ba_31_0_1 = LDS128(lin[2], 19456); SB;
        MFMA(accA[1][4], ba_30_0_4, aa_30_0_1); SB;
        const v4u ba_31_1_1 = LDS128(lin[2], 24576); SB;
        MFMA(accA[1][4], ba_30_1_4, aa_30_1_1); SB;
        WAITL(2); SB;
        MFMA(accA[0][0], ba_31_0_0, aa_31_0_0); SB;
        const v4u ba_31_0_2 = LDS128(lin[2], 20480); SB;
        MFMA(accA[0][0], ba_31_1_0, aa_31_1_0); SB;
        const v4u ba_31_1_2 = LDS128(lin[2], 25600); SB;
        MFMA(accA[1][0], ba_31_0_0, aa_31_0_1); SB;
        const v4u ba_31_0_3 = LDS128(lin[2], 21504); SB;
        MFMA(accA[1][0], ba_31_1_0, aa_31_1_1); SB;
        WAITL(3); SB;
        MFMA(accA[0][1], ba_31_0_1, aa_31_0_0); SB;
        const v4u ba_31_1_3 = LDS128(lin[2], 26624); SB;
        MFMA(accA[0][1], ba_31_1_1, aa_31_1_0); SB;
        const v4u ba_31_0_4 = LDS128(lin[2], 22528); SB;
        MFMA(accA[1][1], ba_31_0_1, aa_31_0_1); SB;
        MFMA(accA[1][1], ba_31_1_1, aa_31_1_1); SB;
        const v4u ba_31_1_4 = LDS128(lin[2], 27648); SB;
        WAITL(4); SB;
        MFMA(accA[0][2], ba_31_0_2, aa_31_0_0); SB;
        MFMA(accA[0][2], ba_31_1_2, aa_31_1_0); SB;
        MFMA(accA[1][2], ba_31_0_2, aa_31_0_1); SB;
        MFMA(accA[1][2], ba_31_1_2, aa_31_1_1); SB;
        WAITL(2); SB;
        MFMA(accA[0][3], ba_31_0_3, aa_31_0_0); SB;
        MFMA(accA[0][3], ba_31_1_3, aa_31_1_0); SB;
        MFMA(accA[1][3], ba_31_0_3, aa_31_0_1); SB;
        MFMA(accA[1][3], ba_31_1_3, aa_31_1_1); SB;
        WAITL(0); SB;
        MFMA(accA[0][4], ba_31_0_4, aa_31_0_0); SB;
        MFMA(accA[0][4], ba_31_1_4, aa_31_1_0); SB;
        MFMA(accA[1][4], ba_31_0_4, aa_31_0_1); SB;
        MFMA(accA[1][4], ba_31_1_4, aa_31_1_1); SB;
        MFMA_END;
        C_EPI_A();
        NEW_LIN();
        BAR(); SB;
        SET_SWZ(0u); INIT5(accD, xr0, xr1, xr2);
        const v4u wd_0 = LDS128(swz0[0], 0); SB;
        const v4u wd_1 = LDS128(swz0[0], 10240); SB;
        const v4u wd_2 = LDS128(swz1[0], 0); SB;
        const v4u wd_3 = LDS128(swz1[0], 10240); SB;
        const v4u wd_4 = LDS128(swz0[0], 2048); SB;
        const v4u wd_5 = LDS128(swz0[0], 12288); SB;
        const v4u wd_6 = LDS128(swz1[0], 2048); SB;
        const v4u wd_7 = LDS128(swz1[0], 12288); SB;
        const v4u wd_8 = LDS128(swz0[0], 4096); SB;
        WAITL(5); SB;
        MFMA(accD[0], wd_0, pq[0]); SB;
        const v4u wd_9 = LDS128(swz0[0], 14336); SB;
        MFMA(accD[0], wd_1, pq[1]); SB;
        const v4u wd_10 = LDS128(swz1[0], 4096); SB;
        MFMA(accD[0], wd_2, pq[2]); SB;
        const v4u wd_11 = LDS128(swz1[0], 14336); SB;
        MFMA(accD[0], wd_3, pq[3]); SB;
        const v4u wd_12 = LDS128(swz0[0], 6144); SB;
        WAITL(5); SB;
        MFMA(accD[1], wd_4, pq[0]); SB;
        const v4u wd_13 = LDS128(swz0[0], 16384); SB;
        MFMA(accD[1], wd_5, pq[1]); SB;
        const v4u wd_14 = LDS128(swz1[0], 6144); SB;
        MFMA(accD[1], wd_6, pq[2]); SB;
        const v4u wd_15 = LDS128(swz1[0], 16384); SB;
        MFMA(accD[1], wd_7, pq[3]); SB;
        const v4u wd_16 = LDS128(swz0[0], 8192); SB;
        WAITL(5); SB;
        MFMA(accD[2], wd_8, pq[0]); SB;
        const v4u wd_17 = LDS128(swz0[0], 18432); SB;
        MFMA(accD[2], wd_9, pq[1]); SB;
        const v4u wd_18 = LDS128(swz1[0], 8192); SB;
        MFMA(accD[2], wd_10, pq[2]); SB;
        const v4u wd_19 = LDS128(swz1[0], 18432); SB;
        MFMA(accD[2], wd_11, pq[3]); SB;
        WAITL(4); SB;
        MFMA(accD[3], wd_12, pq[0]); SB;
        MFMA(accD[3], wd_13, pq[1]); SB;
        MFMA(accD[3], wd_14, pq[2]); SB;
        MFMA(accD[3], wd_15, pq[3]); SB;
        WAITL(0); SB;
        MFMA(accD[4], wd_16, pq[0]); SB;
        MFMA(accD[4], wd_17, pq[1]); SB;
        MFMA(accD[4], wd_18, pq[2]); SB;
        MFMA(accD[4], wd_19, pq[3]); SB;
        DRAIN();
        FLAG_PW(F1_IN);
        C_GEN();
        NEW_LIN();
        BAR(); SB;
        const v4u wd_20 = LDS128(swz0[0], 20480); SB;
        const v4u wd_21 = LDS128(swz0[0], 30720); SB;
        const v4u wd_22 = LDS128(swz1[0], 20480); SB;
        const v4u wd_23 = LDS128(swz1[0], 30720); SB;
        const v4u wd_24 = LDS128(swz0[0], 22528); SB;
        const v4u wd_25 = LDS128(swz0[0], 32768); SB;
        const v4u wd_26 = LDS128(swz1[0], 22528); SB;
        const v4u wd_27 = LDS128(swz1[0], 32768); SB;
        const v4u wd_28 = LDS128(swz0[0], 24576); SB;
        PRE_D();
        __builtin_amdgcn_s_sleep(13);
        POLL_PW(F1_IN);
        pq[4] = LDG(vlin + 0, sPQ + spread(ord(1, cD) * 4096u)); SB;
        pq[5] = LDG(vlin + 1024, sPQ + spread(ord(1, cD) * 4096u)); SB;
        pq[6] = LDG(vlin + 2048, sPQ + spread(ord(1, cD) * 4096u)); SB;
        pq[7] = LDG(vlin + 3072, sPQ + spread(ord(1, cD) * 4096u)); SB;
        pq[8] = LDG(vlin + 0, sPQ + spread(ord(2, cD) * 4096u)); SB;
        pq[9] = LDG(vlin + 1024, sPQ + spread(ord(2, cD) * 4096u)); SB;
        pq[10] = LDG(vlin + 2048, sPQ + spread(ord(2, cD) * 4096u)); SB;
        pq[11] = LDG(vlin + 3072, sPQ + spread(ord(2, cD) * 4096u)); SB;
        pq[12] = LDG(vlin + 0, sPQ + spread(ord(3, cD) * 4096u)); SB;
        pq[13] = LDG(vlin + 1024, sPQ + spread(ord(3, cD) * 4096u)); SB;
        pq[14] = LDG(vlin + 2048, sPQ + spread(ord(3, cD) * 4096u)); SB;
        pq[15] = LDG(vlin + 3072, sPQ + spread(ord(3, cD) * 4096u)); SB;
        WAITL(5); SB;
        MFMA(accD[0], wd_20, pq[4]); SB;
        const v4u wd_29 = LDS128(swz0[0], 34816); SB;
        MFMA(accD[0], wd_21, pq[5]); SB;
        const v4u wd_30 = LDS128(swz1[0], 24576); SB;
        MFMA(accD[0], wd_22, pq[6]); SB;
        const v4u wd_31 = LDS128(swz1[0], 34816); SB;
        MFMA(accD[0], wd_23, pq[7]); SB;
        const v4u wd_32 = LDS128(swz0[0], 26624); SB;
        WAITL(5); SB;
        MFMA(accD[1], wd_24, pq[4]); SB;
        pq[16] = LDG(vlin + 0, sPQ + spread(ord(4, cD) * 4096u)); SB;
        const v4u wd_33 = LDS128(swz0[0], 36864); SB;
        MFMA(accD[1], wd_25, pq[5]); SB;
        const v4u wd_34 = LDS128(swz1[0], 26624); SB;
        MFMA(accD[1], wd_26, pq[6]); SB;
        const v4u wd_35 = LDS128(swz1[0], 36864); SB;
        MFMA(accD[1], wd_27, pq[7]); SB;
        const v4u wd_36 = LDS128(swz0[0], 28672); SB;
        WAITL(5); SB;
        MFMA(accD[2], wd_28, pq[4]); SB;
        const v4u wd_37 = LDS128(swz0[0], 38912); SB;
        MFMA(accD[2], wd_29, pq[5]); SB;
        pq[17] = LDG(vlin + 1024, sPQ + spread(ord(4, cD) * 4096u)); SB;
        const v4u wd_38 = LDS128(swz1[0], 28672); SB;
        MFMA(accD[2], wd_30, pq[6]); SB;
        const v4u wd_39 = LDS128(swz1[0], 38912); SB;
        MFMA(accD[2], wd_31, pq[7]); SB;
        BAR(); SB;
        const v4u wd_40 = LDS128(swz0[0], 40960); SB;
        WAITL(5); SB;
        MFMA(accD[3], wd_32, pq[4]); SB;
        const v4u wd_41 = LDS128(swz0[0], 51200); SB;
        MFMA(accD[3], wd_33, pq[5]); SB;
        const v4u wd_42 = LDS128(swz1[0], 40960); SB;
        MFMA(accD[3], wd_34, pq[6]); SB;
        pq[18] = LDG(vlin + 2048, sPQ + spread(ord(4, cD) * 4096u)); SB;
        const v4u wd_43 = LDS128(swz1[0], 51200); SB;
        MFMA(accD[3], wd_35, pq[7]); SB;
        const v4u wd_44 = LDS128(swz0[0], 43008); SB;
        WAITL(5); SB;
        MFMA(accD[4], wd_36, pq[4]); SB;
        const v4u wd_45 = LDS128(swz0[0], 53248); SB;
        MFMA(accD[4], wd_37, pq[5]); SB;
        const v4u wd_46 = LDS128(swz1[0], 43008); SB;
        MFMA(accD[4], wd_38, pq[6]); SB;
        const v4u wd_47 = LDS128(swz1[0], 53248); SB;
        MFMA(accD[4], wd_39, pq[7]); SB;
        pq[19] = LDG(vlin + 3072, sPQ + spread(ord(4, cD) * 4096u)); SB;
        const v4u wd_48 = LDS128(swz0[0], 45056); SB;
        WAITL(5); SB;
        MFMA(accD[0], wd_40, pq[8]); SB;
        const v4u wd_49 = LDS128(swz0[0], 55296); SB;
        MFMA(accD[0], wd_41, pq[9]); SB;
        const v4u wd_50 = LDS128(swz1[0], 45056); SB;
        MFMA(accD[0], wd_42, pq[10]); SB;
        const v4u wd_51 = LDS128(swz1[0], 55296); SB;
        MFMA(accD[0], wd_43, pq[11]); SB;
        const v4u wd_52 = LDS128(swz0[0], 47104); SB;
        WAITL(5); SB;
        MFMA(accD[1], wd_44, pq[8]); SB;
        pq[20] = LDG(vlin + 0, sPQ + spread(ord(5, cD) * 4096u)); SB;
        const v4u wd_53 = LDS128(swz0[0], 57344); SB;
        MFMA(accD[1], wd_45, pq[9]); SB;
        const v4u wd_54 = LDS128(swz1[0], 47104); SB;
        MFMA(accD[1], wd_46, pq[10]); SB;
        const v4u wd_55 = LDS128(swz1[0], 57344); SB;
        MFMA(accD[1], wd_47, pq[11]); SB;
        const v4u wd_56 = LDS128(swz0[0], 49152); SB;
        WAITL(5); SB;
        MFMA(accD[2], wd_48, pq[8]); SB;
        const v4u wd_57 = LDS128(swz0[0], 59392); SB;
        MFMA(accD[2], wd_49, pq[9]); SB;
        pq[21] = LDG(vlin + 1024, sPQ + spread(ord(5, cD) * 4096u)); SB;
        const v4u wd_58 = LDS128(swz1[0], 49152); SB;
        MFMA(accD[2], wd_50, pq[10]); SB;
        const v4u wd_59 = LDS128(swz1[0], 59392); SB;
        MFMA(accD[2], wd_51, pq[11]); SB;
        BAR(); SB;
        const v4u wd_60 = LDS128(swz0[0], 61440); SB;
        WAITL(5); SB;
        MFMA(accD[3], wd_52, pq[8]); SB;
        const v4u wd_61 = LDS128(swz0[1], 6144); SB;
        MFMA(accD[3], wd_53, pq[9]); SB;
        const v4u wd_62 = LDS128(swz1[0], 61440); SB;
        MFMA(accD[3], wd_54, pq[10]); SB;
        pq[22] = LDG(vlin + 2048, sPQ + spread(ord(5, cD) * 4096u)); SB;
        const v4u wd_63 = LDS128(swz1[1], 6144); SB;
        MFMA(accD[3], wd_55, pq[11]); SB;
        const v4u wd_64 = LDS128(swz0[0], 63488); SB;
        WAITL(5); SB;
        MFMA(accD[4], wd_56, pq[8]); SB;
        const v4u wd_65 = LDS128(swz0[1], 8192); SB;
        MFMA(accD[4], wd_57, pq[9]); SB;
        const v4u wd_66 = LDS128(swz1[0], 63488); SB;
        MFMA(accD[4], wd_58, pq[10]); SB;
        const v4u wd_67 = LDS128(swz1[1], 8192); SB;
        MFMA(accD[4], wd_59, pq[11]); SB;
        pq[23] = LDG(vlin + 3072, sPQ + spread(ord(5, cD) * 4096u)); SB;
        const v4u wd_68 = LDS128(swz0[1], 0); SB;
        WAITL(5); SB;
        MFMA(accD[0], wd_60, pq[12]); SB;
        const v4u wd_69 = LDS128(swz0[1], 10240); SB;
        MFMA(accD[0], wd_61, pq[13]); SB;
        const v4u wd_70 = LDS128(swz1[1], 0); SB;
        MFMA(accD[0], wd_62, pq[14]); SB;
        const v4u wd_71 = LDS128(swz1[1], 10240); SB;
        MFMA(accD[0], wd_63, pq[15]); SB;
        const v4u wd_72 = LDS128(swz0[1], 2048); SB;
        WAITL(5); SB;
        MFMA(accD[1], wd_64, pq[12]); SB;
        pq[24] = LDG(vlin + 0, sPQ + spread(ord(6, cD) * 4096u)); SB;
        const v4u wd_73 = LDS128(swz0[1], 12288); SB;
        MFMA(accD[1], wd_65, pq[13]); SB;
        const v4u wd_74 = LDS128(swz1[1], 2048); SB;
        MFMA(accD[1], wd_66, pq[14]); SB;
        const v4u wd_75 = LDS128(swz1[1], 12288); SB;
        MFMA(accD[1], wd_67, pq[15]); SB;
        const v4u wd_76 = LDS128(swz0[1], 4096); SB;
        WAITL(5); SB;
        MFMA(accD[2], wd_68, pq[12]); SB;
        const v4u wd_77 = LDS128(swz0[1], 14336); SB;
        MFMA(accD[2], wd_69, pq[13]); SB;
        pq[25] = LDG(vlin + 1024, sPQ + spread(ord(6, cD) * 4096u)); SB;
        const v4u wd_78 = LDS128(swz1[1], 4096); SB;
        MFMA(accD[2], wd_70, pq[14]); SB;
        const v4u wd_79 = LDS128(swz1[1], 14336); SB;
        MFMA(accD[2], wd_71, pq[15]); SB;
        BAR(); SB;
        const v4u wd_80 = LDS128(swz0[1], 16384); SB;
        WAITL(5); SB;
        MFMA(accD[3], wd_72, pq[12]); SB;
        const v4u wd_81 = LDS128(swz0[1], 26624); SB;
        MFMA(accD[3], wd_73, pq[13]); SB;
        const v4u wd_82 = LDS128(swz1[1], 16384); SB;
        MFMA(accD[3], wd_74, pq[14]); SB;
        pq[26] = LDG(vlin + 2048, sPQ + spread(ord(6, cD) * 4096u)); SB;
        const v4u wd_83 = LDS128(swz1[1], 26624); SB;
        MFMA(accD[3], wd_75, pq[15]); SB;
        const v4u wd_84 = LDS128(swz0[1], 18432); SB;
        WAITL(5); SB;
        MFMA(accD[4], wd_76, pq[12]); SB;
        const v4u wd_85 = LDS128(swz0[1], 28672); SB;
        MFMA(accD[4], wd_77, pq[13]); SB;
        const v4u wd_86 = LDS128(swz1[1], 18432); SB;
        MFMA(accD[4], wd_78, pq[14]); SB;
        const v4u wd_87 = LDS128(swz1[1], 28672); SB;
        MFMA(accD[4], wd_79, pq[15]); SB;
        pq[27] = LDG(vlin + 3072, sPQ + spread(ord(6, cD) * 4096u)); SB;
        const v4u wd_88 = LDS128(swz0[1], 20480); SB;
        WAITL(5); SB;
        MFMA(accD[0], wd_80, pq[16]); SB;
        const v4u wd_89 = LDS128(swz0[1], 30720); SB;
        MFMA(accD[0], wd_81, pq[17]); SB;
        const v4u wd_90 = LDS128(swz1[1], 20480); SB;
        MFMA(accD[0], wd_82, pq[18]); SB;
        const v4u wd_91 = LDS128(swz1[1], 30720); SB;
        MFMA(accD[0], wd_83, pq[19]); SB;
        const v4u wd_92 = LDS128(swz0[1], 22528); SB;
        WAITL(5); SB;
        MFMA(accD[1], wd_84, pq[16]); SB;
        pq[28] = LDG(vlin + 0, sPQ + spread(ord(7, cD) * 4096u)); SB;
        const v4u wd_93 = LDS128(swz0[1], 32768); SB;
        MFMA(accD[1], wd_85, pq[17]); SB;
        const v4u wd_94 = LDS128(swz1[1], 22528); SB;
        MFMA(accD[1], wd_86, pq[18]); SB;
        const v4u wd_95 = LDS128(swz1[1], 32768); SB;
        MFMA(accD[1], wd_87, pq[19]); SB;
        const v4u wd_96 = LDS128(swz0[1], 24576); SB;
        WAITL(5); SB;
        MFMA(accD[2], wd_88, pq[16]); SB;
        const v4u wd_97 = LDS128(swz0[1], 34816); SB;
        MFMA(accD[2], wd_89, pq[17]); SB;
        pq[29] = LDG(vlin + 1024, sPQ + spread(ord(7, cD) * 4096u)); SB;
        const v4u wd_98 = LDS128(swz1[1], 24576); SB;
        MFMA(accD[2], wd_90, pq[18]); SB;
        const v4u wd_99 = LDS128(swz1[1], 34816); SB;
        MFMA(accD[2], wd_91, pq[19]); SB;
        BAR(); SB;
        const v4u wd_100 = LDS128(swz0[1], 36864); SB;
        WAITL(5); SB;
        MFMA(accD[3], wd_92, pq[16]); SB;
        const v4u wd_101 = LDS128(swz0[1], 47104); SB;
        MFMA(accD[3], wd_93, pq[17]); SB;
        const v4u wd_102 = LDS128(swz1[1], 36864); SB;
        MFMA(accD[3], wd_94, pq[18]); SB;
        pq[30] = LDG(vlin + 2048, sPQ + spread(ord(7, cD) * 4096u)); SB;
        const v4u wd_103 = LDS128(swz1[1], 47104); SB;
        MFMA(accD[3], wd_95, pq[19]); SB;
        const v4u wd_104 = LDS128(swz0[1], 38912); SB;
        WAITL(5); SB;
        MFMA(accD[4], wd_96, pq[16]); SB;
        const v4u wd_105 = LDS128(swz0[1], 49152); SB;
        MFMA(accD[4], wd_97, pq[17]); SB;
        const v4u wd_106 = LDS128(swz1[1], 38912); SB;
        MFMA(accD[4], wd_98, pq[18]); SB;
        const v4u wd_107 = LDS128(swz1[1], 49152); SB;
        MFMA(accD[4], wd_99, pq[19]); SB;
        pq[31] = LDG(vlin + 3072, sPQ + spread(ord(7, cD) * 4096u)); SB;
        const v4u wd_108 = LDS128(swz0[1], 40960); SB;
        WAITL(5); SB;
        MFMA(accD[0], wd_100, pq[20]); SB;
        const v4u wd_109 = LDS128(swz0[1], 51200); SB;
        MFMA(accD[0], wd_101, pq[21]); SB;
        const v4u wd_110 = LDS128(swz1[1], 40960); SB;
        MFMA(accD[0], wd_102, pq[22]); SB;
        const v4u wd_111 = LDS128(swz1[1], 51200); SB;
        MFMA(accD[0], wd_103, pq[23]); SB;
        const v4u wd_112 = LDS128(swz0[1], 43008); SB;
        WAITL(5); SB;
        MFMA(accD[1], wd_104, pq[20]); SB;
        pq[32] = LDG(vlin + 0, sPQ + spread(ord(8, cD) * 4096u)); SB;
        const v4u wd_113 = LDS128(swz0[1], 53248); SB;
        MFMA(accD[1], wd_105, pq[21]); SB;
        const v4u wd_114 = LDS128(swz1[1], 43008); SB;
        MFMA(accD[1], wd_106, pq[22]); SB;
        const v4u wd_115 = LDS128(swz1[1], 53248); SB;
        MFMA(accD[1], wd_107, pq[23]); SB;
        const v4u wd_116 = LDS128(swz0[1], 45056); SB;
        WAITL(5); SB;
        MFMA(accD[2], wd_108, pq[20]); SB;
        const v4u wd_117 = LDS128(swz0[1], 55296); SB;
        MFMA(accD[2], wd_109, pq[21]); SB;
        pq[33] = LDG(vlin + 1024, sPQ + spread(ord(8, cD) * 4096u)); SB;
        const v4u wd_118 = LDS128(swz1[1], 45056); SB;
        MFMA(accD[2], wd_110, pq[22]); SB;
        const v4u wd_119 = LDS128(swz1[1], 55296); SB;
        MFMA(accD[2], wd_111, pq[23]); SB;
        BAR(); SB;
        const v4u wd_120 = LDS128(swz0[1], 57344); SB;
        WAITL(5); SB;
        MFMA(accD[3], wd_112, pq[20]); SB;
        const v4u wd_121 = LDS128(swz0[2], 2048); SB;
        MFMA(accD[3], wd_113, pq[21]); SB;
        const v4u wd_122 = LDS128(swz1[1], 57344); SB;
        MFMA(accD[3], wd_114, pq[22]); SB;
        pq[34] = LDG(vlin + 2048, sPQ + spread(ord(8, cD) * 4096u)); SB;
        const v4u wd_123 = LDS128(swz1[2], 2048); SB;
        MFMA(accD[3], wd_115, pq[23]); SB;
        const v4u wd_124 = LDS128(swz0[1], 59392); SB;
        WAITL(5); SB;
        MFMA(accD[4], wd_116, pq[20]); SB;
        const v4u wd_125 = LDS128(swz0[2], 4096); SB;
        MFMA(accD[4], wd_117, pq[21]); SB;
        const v4u wd_126 = LDS128(swz1[1], 59392); SB;
        MFMA(accD[4], wd_118, pq[22]); SB;
        const v4u wd_127 = LDS128(swz1[2], 4096); SB;
        MFMA(accD[4], wd_119, pq[23]); SB;
        pq[35] = LDG(vlin + 3072, sPQ + spread(ord(8, cD) * 4096u)); SB;
        const v4u wd_128 = LDS128(swz0[1], 61440); SB;
        WAITL(5); SB;
        MFMA(accD[0], wd_120, pq[24]); SB;
        const v4u wd_129 = LDS128(swz0[2], 6144); SB;
        MFMA(accD[0], wd_121, pq[25]); SB;
        const v4u wd_130 = LDS128(swz1[1], 61440); SB;
        MFMA(accD[0], wd_122, pq[26]); SB;
        const v4u wd_131 = LDS128(swz1[2], 6144); SB;
        MFMA(accD[0], wd_123, pq[27]); SB;
        const v4u wd_132 = LDS128(swz0[1], 63488); SB;
        WAITL(5); SB;
        MFMA(accD[1], wd_124, pq[24]); SB;
        pq[36] = LDG(vlin + 0, sPQ + spread(ord(9, cD) * 4096u)); SB;
        const v4u wd_133 = LDS128(swz0[2], 8192); SB;
        MFMA(accD[1], wd_125, pq[25]); SB;
        const v4u wd_134 = LDS128(swz1[1], 63488); SB;
        MFMA(accD[1], wd_126, pq[26]); SB;
        const v4u wd_135 = LDS128(swz1[2], 8192); SB;
        MFMA(accD[1], wd_127, pq[27]); SB;
        const v4u wd_136 = LDS128(swz0[2], 0); SB;
        WAITL(5); SB;
        MFMA(accD[2], wd_128, pq[24]); SB;
        const v4u wd_137 = LDS128(swz0[2], 10240); SB;
        MFMA(accD[2], wd_129, pq[25]); SB;
        pq[37] = LDG(vlin + 1024, sPQ + spread(ord(9, cD) * 4096u)); SB;
        const v4u wd_138 = LDS128(swz1[2], 0); SB;
        MFMA(accD[2], wd_130, pq[26]); SB;
        const v4u wd_139 = LDS128(swz1[2], 10240); SB;
        MFMA(accD[2], wd_131, pq[27]); SB;
        BAR(); SB;
        const v4u wd_140 = LDS128(swz0[0], 0); SB;
        WAITL(5); SB;
        MFMA(accD[3], wd_132, pq[24]); SB;
        const v4u wd_141 = LDS128(swz0[0], 10240); SB;
        MFMA(accD[3], wd_133, pq[25]); SB;
        const v4u wd_142 = LDS128(swz1[0], 0); SB;
        MFMA(accD[3], wd_134, pq[26]); SB;
        pq[38] = LDG(vlin + 2048, sPQ + spread(ord(9, cD) * 4096u)); SB;
        const v4u wd_143 = LDS128(swz1[0], 10240); SB;
        MFMA(accD[3], wd_135, pq[27]); SB;
        const v4u wd_144 = LDS128(swz0[0], 2048); SB;
        WAITL(5); SB;
        MFMA(accD[4], wd_136, pq[24]); SB;
        const v4u wd_145 = LDS128(swz0[0], 12288); SB;
        MFMA(accD[4], wd_137, pq[25]); SB;
        const v4u wd_146 = LDS128(swz1[0], 2048); SB;
        MFMA(accD[4], wd_138, pq[26]); SB;
        const v4u wd_147 = LDS128(swz1[0], 12288); SB;
        MFMA(accD[4], wd_139, pq[27]); SB;
        pq[39] = LDG(vlin + 3072, sPQ + spread(ord(9, cD) * 4096u)); SB;
        const v4u wd_148 = LDS128(swz0[0], 4096); SB;
        WAITL(5); SB;
        MFMA(accD[0], wd_140, pq[28]); SB;
        const v4u wd_149 = LDS128(swz0[0], 14336); SB;
        MFMA(accD[0], wd_141, pq[29]); SB;
        const v4u wd_150 = LDS128(swz1[0], 4096); SB;
        MFMA(accD[0], wd_142, pq[30]); SB;
        const v4u wd_151 = LDS128(swz1[0], 14336); SB;
        MFMA(accD[0], wd_143, pq[31]); SB;
        const v4u wd_152 = LDS128(swz0[0], 6144); SB;
        WAITL(5); SB;
        MFMA(accD[1], wd_144, pq[28]); SB;
        const v4u wd_153 = LDS128(swz0[0], 16384); SB;
        MFMA(accD[1], wd_145, pq[29]); SB;
        const v4u wd_154 = LDS128(swz1[0], 6144); SB;
        MFMA(accD[1], wd_146, pq[30]); SB;
        const v4u wd_155 = LDS128(swz1[0], 16384); SB;
        MFMA(accD[1], wd_147, pq[31]); SB;
        const v4u wd_156 = LDS128(swz0[0], 8192); SB;
        WAITL(5); SB;
        MFMA(accD[2], wd_148, pq[28]); SB;
        const v4u wd_157 = LDS128(swz0[0], 18432); SB;
        MFMA(accD[2], wd_149, pq[29]); SB;
        const v4u wd_158 = LDS128(swz1[0], 8192); SB;
        MFMA(accD[2], wd_150, pq[30]); SB;
        const v4u wd_159 = LDS128(swz1[0], 18432); SB;
        MFMA(accD[2], wd_151, pq[31]); SB;
        BAR(); SB;
        const v4u wd_160 = LDS128(swz0[0], 20480); SB;
        WAITL(5); SB;
        MFMA(accD[3], wd_152, pq[28]); SB;
        const v4u wd_161 = LDS128(swz0[0], 30720); SB;
        MFMA(accD[3], wd_153, pq[29]); SB;
        const v4u wd_162 = LDS128(swz1[0], 20480); SB;
        MFMA(accD[3], wd_154, pq[30]); SB;
        const v4u wd_163 = LDS128(swz1[0], 30720); SB;
        MFMA(accD[3], wd_155, pq[31]); SB;
        const v4u wd_164 = LDS128(swz0[0], 22528); SB;
        WAITL(5); SB;
        MFMA(accD[4], wd_156, pq[28]); SB;
        const v4u wd_165 = LDS128(swz0[0], 32768); SB;
        MFMA(accD[4], wd_157, pq[29]); SB;
        const v4u wd_166 = LDS128(swz1[0], 22528); SB;
        MFMA(accD[4], wd_158, pq[30]); SB;
        const v4u wd_167 = LDS128(swz1[0], 32768); SB;
        MFMA(accD[4], wd_159, pq[31]); SB;
        const v4u wd_168 = LDS128(swz0[0], 24576); SB;
        WAITL(5); SB;
        MFMA(accD[0], wd_160, pq[32]); SB;
        const v4u wd_169 = LDS128(swz0[0], 34816); SB;
        MFMA(accD[0], wd_161, pq[33]); SB;
        const v4u wd_170 = LDS128(swz1[0], 24576); SB;
        MFMA(accD[0], wd_162, pq[34]); SB;
        const v4u wd_171 = LDS128(swz1[0], 34816); SB;
        MFMA(accD[0], wd_163, pq[35]); SB;
        const v4u wd_172 = LDS128(swz0[0], 26624); SB;
        WAITL(5); SB;
        MFMA(accD[1], wd_164, pq[32]); SB;
        const v4u wd_173 = LDS128(swz0[0], 36864); SB;
        MFMA(accD[1], wd_165, pq[33]); SB;
        const v4u wd_174 = LDS128(swz1[0], 26624); SB;
        MFMA(accD[1], wd_166, pq[34]); SB;
        const v4u wd_175 = LDS128(swz1[0], 36864); SB;
        MFMA(accD[1], wd_167, pq[35]); SB;
        const v4u wd_176 = LDS128(swz0[0], 28672); SB;
        WAITL(5); SB;
        MFMA(accD[2], wd_168, pq[32]); SB;
        const v4u wd_177 = LDS128(swz0[0], 38912); SB;
        MFMA(accD[2], wd_169, pq[33]); SB;
        const v4u wd_178 = LDS128(swz1[0], 28672); SB;
        MFMA(accD[2], wd_170, pq[34]); SB;
        const v4u wd_179 = LDS128(swz1[0], 38912); SB;
        MFMA(accD[2], wd_171, pq[35]); SB;
        BAR(); SB;
        const v4u wd_180 = LDS128(swz0[0], 40960); SB;
        WAITL(5); SB;
        MFMA(accD[3], wd_172, pq[32]); SB;
        const v4u wd_181 = LDS128(swz0[0], 51200); SB;
        MFMA(accD[3], wd_173, pq[33]); SB;
        const v4u wd_182 = LDS128(swz1[0], 40960); SB;
        MFMA(accD[3], wd_174, pq[34]); SB;
        const v4u wd_183 = LDS128(swz1[0], 51200); SB;
        MFMA(accD[3], wd_175, pq[35]); SB;
        const v4u wd_184 = LDS128(swz0[0], 43008); SB;
        WAITL(5); SB;
        MFMA(accD[4], wd_176, pq[32]); SB;
        const v4u wd_185 = LDS128(swz0[0], 53248); SB;
        MFMA(accD[4], wd_177, pq[33]); SB;
        const v4u wd_186 = LDS128(swz1[0], 43008); SB;
        MFMA(accD[4], wd_178, pq[34]); SB;
        const v4u wd_187 = LDS128(swz1[0], 53248); SB;
        MFMA(accD[4], wd_179, pq[35]); SB;
        const v4u wd_188 = LDS128(swz0[0], 45056); SB;
        WAITL(5); SB;
        MFMA(accD[0], wd_180, pq[36]); SB;
        const v4u wd_189 = LDS128(swz0[0], 55296); SB;
        MFMA(accD[0], wd_181, pq[37]); SB;
        const v4u wd_190 = LDS128(swz1[0], 45056); SB;
        MFMA(accD[0], wd_182, pq[38]); SB;
        const v4u wd_191 = LDS128(swz1[0], 55296); SB;
        MFMA(accD[0], wd_183, pq[39]); SB;
        const v4u wd_192 = LDS128(swz0[0], 47104); SB;
        WAITL(5); SB;
        MFMA(accD[1], wd_184, pq[36]); SB;
        const v4u wd_193 = LDS128(swz0[0], 57344); SB;
        MFMA(accD[1], wd_185, pq[37]); SB;
        const v4u wd_194 = LDS128(swz1[0], 47104); SB;
        MFMA(accD[1], wd_186, pq[38]); SB;
        const v4u wd_195 = LDS128(swz1[0], 57344); SB;
        MFMA(accD[1], wd_187, pq[39]); SB;
        const v4u wd_196 = LDS128(swz0[0], 49152); SB;
        WAITL(5); SB;
        MFMA(accD[2], wd_188, pq[36]); SB;
        const v4u wd_197 = LDS128(swz0[0], 59392); SB;
        MFMA(accD[2], wd_189, pq[37]); SB;
        const v4u wd_198 = LDS128(swz1[0], 49152); SB;
        MFMA(accD[2], wd_190, pq[38]); SB;
        const v4u wd_199 = LDS128(swz1[0], 59392); SB;
        MFMA(accD[2], wd_191, pq[39]); SB;
        WAITL(4); SB;
        MFMA(accD[3], wd_192, pq[36]); SB;
        MFMA(accD[3], wd_193, pq[37]); SB;
        MFMA(accD[3], wd_194, pq[38]); SB;
        MFMA(accD[3], wd_195, pq[39]); SB;
        WAITL(0); SB;
        MFMA(accD[4], wd_196, pq[36]); SB;
        MFMA(accD[4], wd_197, pq[37]); SB;
        MFMA(accD[4], wd_198, pq[38]); SB;
        MFMA(accD[4], wd_199, pq[39]); SB;
        MFMA_END;
        C_EPI_D(); hf[0] = hk0; hf[1] = hk1;
        NEW_LIN();
        BAR(); SB;
        const v4u we_0 = LDS128(lin[0], 61440); SB;
        const v4u we_1 = LDS128(lin[1], 2048); SB;
        const v4u we_2 = LDS128(lin[0], 62464); SB;
        const v4u we_3 = LDS128(lin[1], 3072); SB;
        const v4u we_4 = LDS128(lin[0], 63488); SB;
        const v4u we_5 = LDS128(lin[1], 4096); SB;
        const v4u we_6 = LDS128(lin[0], 64512); SB;
        const v4u we_7 = LDS128(lin[1], 5120); SB;
        const v4u we_8 = LDS128(lin[1], 0); SB;
        WAITL(7); SB;
        MFMA0(accE[0], we_0, hf[0]); SB;
        const v4u we_9 = LDS128(lin[1], 6144); SB;
        MFMA(accE[0], we_1, hf[1]); SB;
        const v4u we_10 = LDS128(lin[1], 1024); SB;
        WAITL(7); SB;
        MFMA0(accE[1], we_2, hf[0]); SB;
        const v4u we_11 = LDS128(lin[1], 7168); SB;
        MFMA(accE[1], we_3, hf[1]); SB;
        WAITL(6); SB;
        MFMA0(accE[2], we_4, hf[0]); SB;
        MFMA(accE[2], we_5, hf[1]); SB;
        WAITL(4); SB;
        MFMA0(accE[3], we_6, hf[0]); SB;
        MFMA(accE[3], we_7, hf[1]); SB;
        WAITL(2); SB;
        MFMA0(accE[4], we_8, hf[0]); SB;
        MFMA(accE[4], we_9, hf[1]); SB;
        WAITL(0); SB;
        MFMA0(accE[5], we_10, hf[0]); SB;
        MFMA(accE[5], we_11, hf[1]); SB;
        DRAIN();
        FLAG_PW(F2_IN);
        C_COLSUM();
        NEW_LIN();
        BAR(); SB;
        C_RD_CS(accG, 0);
        const v4u we_12 = LDS128(lin[1], 8192); SB;
        const v4u we_13 = LDS128(lin[1], 14336); SB;
        const v4u we_14 = LDS128(lin[1], 9216); SB;
        const v4u we_15 = LDS128(lin[1], 15360); SB;
        const v4u we_16 = LDS128(lin[1], 10240); SB;
        const v4u we_17 = LDS128(lin[1], 16384); SB;
        const v4u we_18 = LDS128(lin[1], 11264); SB;
        const v4u we_19 = LDS128(lin[1], 17408); SB;
        const v4u we_20 = LDS128(lin[1], 12288); SB;
        PRE_E();
        __builtin_amdgcn_s_sleep(8);
        POLL_PW(F2_IN);
        hf[2] = LDG(vlin + 0, sH + spread(ord(1, cE) * 2048u)); SB;
        hf[3] = LDG(vlin + 1024, sH + spread(ord(1, cE) * 2048u)); SB;
        hf[4] = LDG(vlin + 0, sH + spread(ord(2, cE) * 2048u)); SB;
        hf[5] = LDG(vlin + 1024, sH + spread(ord(2, cE) * 2048u)); SB;
        hf[6] = LDG(vlin + 0, sH + spread(ord(3, cE) * 2048u)); SB;
        hf[7] = LDG(vlin + 1024, sH + spread(ord(3, cE) * 2048u)); SB;
        hf[8] = LDG(vlin + 0, sH + spread(ord(4, cE) * 2048u)); SB;
        hf[9] = LDG(vlin + 1024, sH + spread(ord(4, cE) * 2048u)); SB;
        hf[10] = LDG(vlin + 0, sH + spread(ord(5, cE) * 2048u)); SB;
        hf[11] = LDG(vlin + 1024, sH + spread(ord(5, cE) * 2048u)); SB;
        hf[12] = LDG(vlin + 0, sH + spread(ord(6, cE) * 2048u)); SB;
        hf[13] = LDG(vlin + 1024, sH + spread(ord(6, cE) * 2048u)); SB;
        C_LOAD_ST2(); SB;
        WAITL(7); SB;
        MFMA(accE[0], we_12, hf[2]); SB;
        const v4u we_21 = LDS128(lin[1], 18432); SB;
        MFMA(accE[0], we_13, hf[3]); SB;
        const v4u we_22 = LDS128(lin[1], 13312); SB;
        WAITL(7); SB;
        MFMA(accE[1], we_14, hf[2]); SB;
        const v4u we_23 = LDS128(lin[1], 19456); SB;
        MFMA(accE[1], we_15, hf[3]); SB;
        const v4u we_24 = LDS128(lin[1], 20480); SB;
        WAITL(7); SB;
        MFMA(accE[2], we_16, hf[2]); SB;
        const v4u we_25 = LDS128(lin[1], 26624); SB;
        MFMA(accE[2], we_17, hf[3]); SB;
        hf[14] = LDG(vlin + 0, sH + spread(ord(7, cE) * 2048u)); SB;
        const v4u we_26 = LDS128(lin[1], 21504); SB;
        WAITL(7); SB;
        MFMA(accE[3], we_18, hf[2]); SB;
        const v4u we_27 = LDS128(lin[1], 27648); SB;
        MFMA(accE[3], we_19, hf[3]); SB;
        const v4u we_28 = LDS128(lin[1], 22528); SB;
        WAITL(7); SB;
        MFMA(accE[4], we_20, hf[2]); SB;
        const v4u we_29 = LDS128(lin[1], 28672); SB;
        MFMA(accE[4], we_21, hf[3]); SB;
        const v4u we_30 = LDS128(lin[1], 23552); SB;
        WAITL(7); SB;
        MFMA(accE[5], we_22, hf[2]); SB;
        const v4u we_31 = LDS128(lin[1], 29696); SB;
        MFMA(accE[5], we_23, hf[3]); SB;
        hf[15] = LDG(vlin + 1024, sH + spread(ord(7, cE) * 2048u)); SB;
        const v4u we_32 = LDS128(lin[1], 24576); SB;
        WAITL(7); SB;
        MFMA(accE[0], we_24, hf[4]); SB;
        const v4u we_33 = LDS128(lin[1], 30720); SB;
        MFMA(accE[0], we_25, hf[5]); SB;
        const v4u we_34 = LDS128(lin[1], 25600); SB;
        WAITL(7); SB;
        MFMA(accE[1], we_26, hf[4]); SB;
        const v4u we_35 = LDS128(lin[1], 31744); SB;
        MFMA(accE[1], we_27, hf[5]); SB;
        BAR(); SB;
        const v4u we_36 = LDS128(lin[1], 32768); SB;
        WAITL(7); SB;
        MFMA(accE[2], we_28, hf[4]); SB;
        const v4u we_37 = LDS128(lin[1], 38912); SB;
        MFMA(accE[2], we_29, hf[5]); SB;
        hf[16] = LDG(vlin + 0, sH + spread(ord(8, cE) * 2048u)); SB;
        const v4u we_38 = LDS128(lin[1], 33792); SB;
        WAITL(7); SB;
        MFMA(accE[3], we_30, hf[4]); SB;
        const v4u we_39 = LDS128(lin[1], 39936); SB;
        MFMA(accE[3], we_31, hf[5]); SB;
        const v4u we_40 = LDS128(lin[1], 34816); SB;
        WAITL(7); SB;
        MFMA(accE[4], we_32, hf[4]); SB;
        const v4u we_41 = LDS128(lin[1], 40960); SB;
        MFMA(accE[4], we_33, hf[5]); SB;
        const v4u we_42 = LDS128(lin[1], 35840); SB;
        WAITL(7); SB;
        MFMA(accE[5], we_34, hf[4]); SB;
        const v4u we_43 = LDS128(lin[1], 41984); SB;
        MFMA(accE[5], we_35, hf[5]); SB;
        hf[17] = LDG(vlin + 1024, sH + spread(ord(8, cE) * 2048u)); SB;
        const v4u we_44 = LDS128(lin[1], 36864); SB;
        WAITL(7); SB;
        MFMA(accE[0], we_36, hf[6]); SB;
        const v4u we_45 = LDS128(lin[1], 43008); SB;
        MFMA(accE[0], we_37, hf[7]); SB;
        const v4u we_46 = LDS128(lin[1], 37888); SB;
        WAITL(7); SB;
        MFMA(accE[1], we_38, hf[6]); SB;
        const v4u we_47 = LDS128(lin[1], 44032); SB;
        MFMA(accE[1], we_39, hf[7]); SB;
        const v4u we_48 = LDS128(lin[1], 45056); SB;
        WAITL(7); SB;
        MFMA(accE[2], we_40, hf[6]); SB;
        const v4u we_49 = LDS128(lin[1], 51200); SB;
        MFMA(accE[2], we_41, hf[7]); SB;
        hf[18] = LDG(vlin + 0, sH + spread(ord(9, cE) * 2048u)); SB;
        const v4u we_50 = LDS128(lin[1], 46080); SB;
        WAITL(7); SB;
        MFMA(accE[3], we_42, hf[6]); SB;
        const v4u we_51 = LDS128(lin[1], 52224); SB;
        MFMA(accE[3], we_43, hf[7]); SB;
        const v4u we_52 = LDS128(lin[1], 47104); SB;
        WAITL(7); SB;
        MFMA(accE[4], we_44, hf[6]); SB;
        const v4u we_53 = LDS128(lin[1], 53248); SB;
        MFMA(accE[4], we_45, hf[7]); SB;
        const v4u we_54 = LDS128(lin[1], 48128); SB;
        WAITL(7); SB;
        MFMA(accE[5], we_46, hf[6]); SB;
        const v4u we_55 = LDS128(lin[1], 54272); SB;
        MFMA(accE[5], we_47, hf[7]); SB;
        hf[19] = LDG(vlin + 1024, sH + spread(ord(9, cE) * 2048u)); SB;
        const v4u we_56 = LDS128(lin[1], 49152); SB;
        WAITL(7); SB;
        MFMA(accE[0], we_48, hf[8]); SB;
        const v4u we_57 = LDS128(lin[1], 55296); SB;
        MFMA(accE[0], we_49, hf[9]); SB;
        const v4u we_58 = LDS128(lin[1], 50176); SB;
        WAITL(7); SB;
        MFMA(accE[1], we_50, hf[8]); SB;
        const v4u we_59 = LDS128(lin[1], 56320); SB;
        MFMA(accE[1], we_51, hf[9]); SB;
        BAR(); SB;
        const v4u we_60 = LDS128(lin[1], 57344); SB;
        WAITL(7); SB;
        MFMA(accE[2], we_52, hf[8]); SB;
        const v4u we_61 = LDS128(lin[1], 63488); SB;
        MFMA(accE[2], we_53, hf[9]); SB;
        const v4u we_62 = LDS128(lin[1], 58368); SB;
        WAITL(7); SB;
        MFMA(accE[3], we_54, hf[8]); SB;
        const v4u we_63 = LDS128(lin[1], 64512); SB;
        MFMA(accE[3], we_55, hf[9]); SB;
        const v4u we_64 = LDS128(lin[1], 59392); SB;
        WAITL(7); SB;
        MFMA(accE[4], we_56, hf[8]); SB;
        const v4u we_65 = LDS128(lin[2], 0); SB;
        MFMA(accE[4], we_57, hf[9]); SB;
        const v4u we_66 = LDS128(lin[1], 60416); SB;
        WAITL(7); SB;
        MFMA(accE[5], we_58, hf[8]); SB;
        const v4u we_67 = LDS128(lin[2], 1024); SB;
        MFMA(accE[5], we_59, hf[9]); SB;
        const v4u we_68 = LDS128(lin[1], 61440); SB;
        WAITL(7); SB;
        MFMA(accE[0], we_60, hf[10]); SB;
        const v4u we_69 = LDS128(lin[2], 2048); SB;
        MFMA(accE[0], we_61, hf[11]); SB;
        const v4u we_70 = LDS128(lin[1], 62464); SB;
        WAITL(7); SB;
        MFMA(accE[1], we_62, hf[10]); SB;
        const v4u we_71 = LDS128(lin[2], 3072); SB;
        MFMA(accE[1], we_63, hf[11]); SB;
        const v4u we_72 = LDS128(lin[0], 0); SB;
        WAITL(7); SB;
        MFMA(accE[2], we_64, hf[10]); SB;
        const v4u we_73 = LDS128(lin[0], 6144); SB;
        MFMA(accE[2], we_65, hf[11]); SB;
        const v4u we_74 = LDS128(lin[0], 1024); SB;
        WAITL(7); SB;
        MFMA(accE[3], we_66, hf[10]); SB;
        const v4u we_75 = LDS128(lin[0], 7168); SB;
        MFMA(accE[3], we_67, hf[11]); SB;
        const v4u we_76 = LDS128(lin[0], 2048); SB;
        WAITL(7); SB;
        MFMA(accE[4], we_68, hf[10]); SB;
        const v4u we_77 = LDS128(lin[0], 8192); SB;
        MFMA(accE[4], we_69, hf[11]); SB;
        const v4u we_78 = LDS128(lin[0], 3072); SB;
        WAITL(7); SB;
        MFMA(accE[5], we_70, hf[10]); SB;
        const v4u we_79 = LDS128(lin[0], 9216); SB;
        MFMA(accE[5], we_71, hf[11]); SB;
        const v4u we_80 = LDS128(lin[0], 4096); SB;
        WAITL(7); SB;
        MFMA(accE[0], we_72, hf[12]); SB;
        const v4u we_81 = LDS128(lin[0], 10240); SB;
        MFMA(accE[0], we_73, hf[13]); SB;
        const v4u we_82 = LDS128(lin[0], 5120); SB;
        WAITL(7); SB;
        MFMA(accE[1], we_74, hf[12]); SB;
        const v4u we_83 = LDS128(lin[0], 11264); SB;
        MFMA(accE[1], we_75, hf[13]); SB;
        BAR(); SB;
        const v4u we_84 = LDS128(lin[0], 12288); SB;
        WAITL(7); SB;
        MFMA(accE[2], we_76, hf[12]); SB;
        const v4u we_85 = LDS128(lin[0], 18432); SB;
        MFMA(accE[2], we_77, hf[13]); SB;
        const v4u we_86 = LDS128(lin[0], 13312); SB;
        WAITL(7); SB;
        MFMA(accE[3], we_78, hf[12]); SB;
        const v4u we_87 = LDS128(lin[0], 19456); SB;
        MFMA(accE[3], we_79, hf[13]); SB;
        const v4u we_88 = LDS128(lin[0], 14336); SB;
        WAITL(7); SB;
        MFMA(accE[4], we_80, hf[12]); SB;
        const v4u we_89 = LDS128(lin[0], 20480); SB;
        MFMA(accE[4], we_81, hf[13]); SB;
        const v4u we_90 = LDS128(lin[0], 15360); SB;
        WAITL(7); SB;
        MFMA(accE[5], we_82, hf[12]); SB;
        const v4u we_91 = LDS128(lin[0], 21504); SB;
        MFMA(accE[5], we_83, hf[13]); SB;
        const v4u we_92 = LDS128(lin[0], 16384); SB;
        WAITL(7); SB;
        MFMA(accE[0], we_84, hf[14]); SB;
        const v4u we_93 = LDS128(lin[0], 22528); SB;
        MFMA(accE[0], we_85, hf[15]); SB;
        const v4u we_94 = LDS128(lin[0], 17408); SB;
        WAITL(7); SB;
        MFMA(accE[1], we_86, hf[14]); SB;
        const v4u we_95 = LDS128(lin[0], 23552); SB;
        MFMA(accE[1], we_87, hf[15]); SB;
        const v4u we_96 = LDS128(lin[0], 24576); SB;
        WAITL(7); SB;
        MFMA(accE[2], we_88, hf[14]); SB;
        const v4u we_97 = LDS128(lin[0], 30720); SB;
        MFMA(accE[2], we_89, hf[15]); SB;
        const v4u we_98 = LDS128(lin[0], 25600); SB;
        WAITL(7); SB;
        MFMA(accE[3], we_90, hf[14]); SB;
        C_ST2();
        const v4u we_99 = LDS128(lin[0], 31744); SB;
        MFMA(accE[3], we_91, hf[15]); SB;
        const v4u we_100 = LDS128(lin[0], 26624); SB;
        WAITL(7); SB;
        MFMA(accE[4], we_92, hf[14]); SB;
        const v4u we_101 = LDS128(lin[0], 32768); SB;
        MFMA(accE[4], we_93, hf[15]); SB;
        const v4u we_102 = LDS128(lin[0], 27648); SB;
        WAITL(7); SB;
        MFMA(accE[5], we_94, hf[14]); SB;
        const v4u we_103 = LDS128(lin[0], 33792); SB;
        MFMA(accE[5], we_95, hf[15]); SB;
        const v4u we_104 = LDS128(lin[0], 28672); SB;
        WAITL(7); SB;
        MFMA(accE[0], we_96, hf[16]); SB;
        const v4u we_105 = LDS128(lin[0], 34816); SB;
        MFMA(accE[0], we_97, hf[17]); SB;
        const v4u we_106 = LDS128(lin[0], 29696); SB;
        WAITL(7); SB;
        MFMA(accE[1], we_98, hf[16]); SB;
        const v4u we_107 = LDS128(lin[0], 35840); SB;
        MFMA(accE[1], we_99, hf[17]); SB;
        BAR(); SB;
        const v4u we_108 = LDS128(lin[0], 36864); SB;
        WAITL(7); SB;
        MFMA(accE[2], we_100, hf[16]); SB;
        C_ST2B();
        const v4u we_109 = LDS128(lin[0], 43008); SB;
        MFMA(accE[2], we_101, hf[17]); SB;
        const v4u we_110 = LDS128(lin[0], 37888); SB;
        WAITL(7); SB;
        MFMA(accE[3], we_102, hf[16]); SB;
        const v4u we_111 = LDS128(lin[0], 44032); SB;
        MFMA(accE[3], we_103, hf[17]); SB;
        const v4u we_112 = LDS128(lin[0], 38912); SB;
        WAITL(7); SB;
        MFMA(accE[4], we_104, hf[16]); SB;
        const v4u we_113 = LDS128(lin[0], 45056); SB;
        MFMA(accE[4], we_105, hf[17]); SB;
        const v4u we_114 = LDS128(lin[0], 39936); SB;
        WAITL(7); SB;
        MFMA(accE[5], we_106, hf[16]); SB;
        const v4u we_115 = LDS128(lin[0], 46080); SB;
        MFMA(accE[5], we_107, hf[17]); SB;
        const v4u we_116 = LDS128(lin[0], 40960); SB;
        WAITL(7); SB;
        MFMA(accE[0], we_108, hf[18]); SB;
        const v4u we_117 = LDS128(lin[0], 47104); SB;
        MFMA(accE[0], we_109, hf[19]); SB;
        const v4u we_118 = LDS128(lin[0], 41984); SB;
        WAITL(7); SB;
        MFMA(accE[1], we_110, hf[18]); SB;
        const v4u we_119 = LDS128(lin[0], 48128); SB;
        MFMA(accE[1], we_111, hf[19]); SB;
        WAITL(6); SB;
        MFMA(accE[2], we_112, hf[18]); SB;
        MFMA(accE[2], we_113, hf[19]); SB;
        WAITL(4); SB;
        MFMA(accE[3], we_114, hf[18]); SB;
        MFMA(accE[3], we_115, hf[19]); SB;
        WAITL(2); SB;
        MFMA(accE[4], we_116, hf[18]); SB;
        WAITL0; BAR(); SB;
        MFMA(accE[4], we_117, hf[19]); SB;
        WAITL(0); SB;
        MFMA(accE[5], we_118, hf[18]); SB;
        MFMA(accE[5], we_119, hf[19]); SB;
        MFMA_END;
        C_EPI_E1();
        NEW_LIN();
        BAR(); SB;
        C_RD_CS(accG, 1);
        const v4u wg_0 = LDS128(lin[0], 49152); SB;
        const v4u wg_1 = LDS128(lin[0], 55296); SB;
        const v4u wg_2 = LDS128(lin[0], 50176); SB;
        const v4u wg_3 = LDS128(lin[0], 56320); SB;
        const v4u wg_4 = LDS128(lin[0], 51200); SB;
        const v4u wg_5 = LDS128(lin[0], 57344); SB;
        const v4u wg_6 = LDS128(lin[0], 52224); SB;
        const v4u wg_7 = LDS128(lin[0], 58368); SB;
        const v4u wg_8 = LDS128(lin[0], 53248); SB;
        C_COR(accG); C_PT_RD(4, 0u + w * 2048u); C_PT_RD(5, 8192u + w * 2048u); C_PT_RD(6, 16384u + w * 2048u);
        WAITL(7); SB;
        MFMA(accG[0], wg_0, hf[0]); SB;
        const v4u wg_9 = LDS128(lin[0], 59392); SB;
        MFMA(accG[0], wg_1, hf[1]); SB;
        const v4u wg_10 = LDS128(lin[0], 54272); SB;
        WAITL(7); SB;
        MFMA(accG[1], wg_2, hf[0]); SB;
        const v4u wg_11 = LDS128(lin[0], 60416); SB;
        MFMA(accG[1], wg_3, hf[1]); SB;
        const v4u wg_12 = LDS128(lin[0], 61440); SB;
        WAITL(7); SB;
        MFMA(accG[2], wg_4, hf[0]); SB;
        const v4u wg_13 = LDS128(lin[1], 2048); SB;
        MFMA(accG[2], wg_5, hf[1]); SB;
        const v4u wg_14 = LDS128(lin[0], 62464); SB;
        WAITL(7); SB;
        MFMA(accG[3], wg_6, hf[0]); SB;
        const v4u wg_15 = LDS128(lin[1], 3072); SB;
        MFMA(accG[3], wg_7, hf[1]); SB;
        const v4u wg_16 = LDS128(lin[0], 63488); SB;
        WAITL(7); SB;
        MFMA(accG[4], wg_8, hf[0]); SB;
        const v4u wg_17 = LDS128(lin[1], 4096); SB;
        MFMA(accG[4], wg_9, hf[1]); SB;
        const v4u wg_18 = LDS128(lin[0], 64512); SB;
        WAITL(7); SB;
        MFMA(accG[5], wg_10, hf[0]); SB;
        const v4u wg_19 = LDS128(lin[1], 5120); SB;
        MFMA(accG[5], wg_11, hf[1]); SB;
        const v4u wg_20 = LDS128(lin[1], 0); SB;
        WAITL(7); SB;
        MFMA(accG[0], wg_12, hf[2]); SB;
        const v4u wg_21 = LDS128(lin[1], 6144); SB;
        MFMA(accG[0], wg_13, hf[3]); SB;
        const v4u wg_22 = LDS128(lin[1], 1024); SB;
        WAITL(7); SB;
        MFMA(accG[1], wg_14, hf[2]); SB;
        const v4u wg_23 = LDS128(lin[1], 7168); SB;
        MFMA(accG[1], wg_15, hf[3]); SB;
        const v4u wg_24 = LDS128(lin[1], 8192); SB;
        WAITL(7); SB;
        MFMA(accG[2], wg_16, hf[2]); SB;
        const v4u wg_25 = LDS128(lin[1], 14336); SB;
        MFMA(accG[2], wg_17, hf[3]); SB;
        const v4u wg_26 = LDS128(lin[1], 9216); SB;
        WAITL(7); SB;
        MFMA(accG[3], wg_18, hf[2]); SB;
        C_G3_ASK(); BAR(); SB;
        const v4u wg_27 = LDS128(lin[1], 15360); SB;
        MFMA(accG[3], wg_19, hf[3]); SB;
        const v4u wg_28 = LDS128(lin[1], 10240); SB;
        WAITL(7); SB;
        MFMA(accG[4], wg_20, hf[2]); SB;
        const v4u wg_29 = LDS128(lin[1], 16384); SB;
        MFMA(accG[4], wg_21, hf[3]); SB;
        const v4u wg_30 = LDS128(lin[1], 11264); SB;
        WAITL(7); SB;
        MFMA(accG[5], wg_22, hf[2]); SB;
        const v4u wg_31 = LDS128(lin[1], 17408); SB;
        MFMA(accG[5], wg_23, hf[3]); SB;
        const v4u wg_32 = LDS128(lin[1], 12288); SB;
        WAITL(7); SB;
        MFMA(accG[0], wg_24, hf[4]); SB;
        const v4u wg_33 = LDS128(lin[1], 18432); SB;
        MFMA(accG[0], wg_25, hf[5]); SB;
        const v4u wg_34 = LDS128(lin[1], 13312); SB;
        WAITL(7); SB;
        MFMA(accG[1], wg_26, hf[4]); SB;
        const v4u wg_35 = LDS128(lin[1], 19456); SB;
        MFMA(accG[1], wg_27, hf[5]); SB;
        WAITL(6); SB;
        MFMA(accG[2], wg_28, hf[4]); SB;
        MFMA(accG[2], wg_29, hf[5]); SB;
        WAITL(4); SB;
        MFMA(accG[3], wg_30, hf[4]); SB;
        MFMA(accG[3], wg_31, hf[5]); SB;
        WAITL(2); SB;
        MFMA(accG[4], wg_32, hf[4]); SB;
        MFMA(accG[4], wg_33, hf[5]); SB;
        WAITL(0); SB;
        MFMA(accG[5], wg_34, hf[4]); SB;
        MFMA(accG[5], wg_35, hf[5]); SB;
        C_PT_RD(0, SCR_LDS + w * 4096u); C_PT_RD(1, SCR_LDS + w * 4096u + 2048u);
        C_EPI_E2();
        NEW_LIN();
        BAR(); SB;
        C_PT_RD(2, 36864u + w * 2048u); C_PT_RD(3, 135168u + w * 2048u);
        const v4u wg_36 = LDS128(lin[1], 20480); SB;
        const v4u wg_37 = LDS128(lin[1], 26624); SB;
        const v4u wg_38 = LDS128(lin[1], 21504); SB;
        const v4u wg_39 = LDS128(lin[1], 27648); SB;
        const v4u wg_40 = LDS128(lin[1], 22528); SB;
        const v4u wg_41 = LDS128(lin[1], 28672); SB;
        const v4u wg_42 = LDS128(lin[1], 23552); SB;
        const v4u wg_43 = LDS128(lin[1], 29696); SB;
        const v4u wg_44 = LDS128(lin[1], 24576); SB;
        WAITL(7); SB;
        MFMA(accG[0], wg_36, hf[6]); SB;
        const v4u wg_45 = LDS128(lin[1], 30720); SB;
        MFMA(accG[0], wg_37, hf[7]); SB;
        const v4u wg_46 = LDS128(lin[1], 25600); SB;
        WAITL(7); SB;
        MFMA(accG[1], wg_38, hf[6]); SB;
        const v4u wg_47 = LDS128(lin[1], 31744); SB;
        MFMA(accG[1], wg_39, hf[7]); SB;
        const v4u wg_48 = LDS128(lin[1], 32768); SB;
        WAITL(7); SB;
        MFMA(accG[2], wg_40, hf[6]); SB;
        const v4u wg_49 = LDS128(lin[1], 38912); SB;
        MFMA(accG[2], wg_41, hf[7]); SB;
        const v4u wg_50 = LDS128(lin[1], 33792); SB;
        WAITL(7); SB;
        MFMA(accG[3], wg_42, hf[6]); SB;
        const v4u wg_51 = LDS128(lin[1], 39936); SB;
        MFMA(accG[3], wg_43, hf[7]); SB;
        const v4u wg_52 = LDS128(lin[1], 34816); SB;
        WAITL(7); SB;
        MFMA(accG[4], wg_44, hf[6]); SB;
        const v4u wg_53 = LDS128(lin[1], 40960); SB;
        MFMA(accG[4], wg_45, hf[7]); SB;
        const v4u wg_54 = LDS128(lin[1], 35840); SB;
        WAITL(7); SB;
        MFMA(accG[5], wg_46, hf[6]); SB;
        const v4u wg_55 = LDS128(lin[1], 41984); SB;
        MFMA(accG[5], wg_47, hf[7]); SB;
        const v4u wg_56 = LDS128(lin[1], 36864); SB;
        WAITL(7); SB;
        MFMA(accG[0], wg_48, hf[8]); SB;
        const v4u wg_57 = LDS128(lin[1], 43008); SB;
        MFMA(accG[0], wg_49, hf[9]); SB;
        const v4u wg_58 = LDS128(lin[1], 37888); SB;
        WAITL(7); SB;
        MFMA(accG[1], wg_50, hf[8]); SB;
        const v4u wg_59 = LDS128(lin[1], 44032); SB;
        MFMA(accG[1], wg_51, hf[9]); SB;
        const v4u wg_60 = LDS128(lin[1], 45056); SB;
        WAITL(7); SB;
        MFMA(accG[2], wg_52, hf[8]); SB;
        const v4u wg_61 = LDS128(lin[1], 51200); SB;
        MFMA(accG[2], wg_53, hf[9]); SB;
        const v4u wg_62 = LDS128(lin[1], 46080); SB;
        WAITL(7); SB;
        MFMA(accG[3], wg_54, hf[8]); SB;
        const v4u wg_63 = LDS128(lin[1], 52224); SB;
        MFMA(accG[3], wg_55, hf[9]); SB;
        const v4u wg_64 = LDS128(lin[1], 47104); SB;
        WAITL(7); SB;
        MFMA(accG[4], wg_56, hf[8]); SB;
        const v4u wg_65 = LDS128(lin[1], 53248); SB;
        MFMA(accG[4], wg_57, hf[9]); SB;
        const v4u wg_66 = LDS128(lin[1], 48128); SB;
        WAITL(7); SB;
        MFMA(accG[5], wg_58, hf[8]); SB;
        const v4u wg_67 = LDS128(lin[1], 54272); SB;
        MFMA(accG[5], wg_59, hf[9]); SB;
        const v4u wg_68 = LDS128(lin[1], 49152); SB;
        WAITL(7); SB;
        MFMA(accG[0], wg_60, hf[10]); SB;
        const v4u wg_69 = LDS128(lin[1], 55296); SB;
        MFMA(accG[0], wg_61, hf[11]); SB;
        const v4u wg_70 = LDS128(lin[1], 50176); SB;
        WAITL(7); SB;
        MFMA(accG[1], wg_62, hf[10]); SB;
        const v4u wg_71 = LDS128(lin[1], 56320); SB;
        MFMA(accG[1], wg_63, hf[11]); SB;
        const v4u wg_72 = LDS128(lin[1], 57344); SB;
        WAITL(7); SB;
        MFMA(accG[2], wg_64, hf[10]); SB;
        const v4u wg_73 = LDS128(lin[1], 63488); SB;
        MFMA(accG[2], wg_65, hf[11]); SB;
        const v4u wg_74 = LDS128(lin[1], 58368); SB;
        WAITL(7); SB;
        MFMA(accG[3], wg_66, hf[10]); SB;
        const v4u wg_75 = LDS128(lin[1], 64512); SB;
        MFMA(accG[3], wg_67, hf[11]); SB;
        const v4u wg_76 = LDS128(lin[1], 59392); SB;
        WAITL(7); SB;
        MFMA(accG[4], wg_68, hf[10]); SB;
        const v4u wg_77 = LDS128(lin[2], 0); SB;
        MFMA(accG[4], wg_69, hf[11]); SB;
        const v4u wg_78 = LDS128(lin[1], 60416); SB;
        WAITL(7); SB;
        MFMA(accG[5], wg_70, hf[10]); SB;
        const v4u wg_79 = LDS128(lin[2], 1024); SB;
        MFMA(accG[5], wg_71, hf[11]); SB;
        const v4u wg_80 = LDS128(lin[1], 61440); SB;
        WAITL(7); SB;
        MFMA(accG[0], wg_72, hf[12]); SB;
        const v4u wg_81 = LDS128(lin[2], 2048); SB;
        MFMA(accG[0], wg_73, hf[13]); SB;
        const v4u wg_82 = LDS128(lin[1], 62464); SB;
        WAITL(7); SB;
        MFMA(accG[1], wg_74, hf[12]); SB;
        const v4u wg_83 = LDS128(lin[2], 3072); SB;
        MFMA(accG[1], wg_75, hf[13]); SB;
        DRAIN(); WAITL0;
        BAR(); SB;
        const v4u wg_84 = LDS128(lin[0], 0); SB;
        WAITL(7); SB;
        MFMA(accG[2], wg_76, hf[12]); SB;
        const v4u wg_85 = LDS128(lin[0], 6144); SB;
        MFMA(accG[2], wg_77, hf[13]); SB;
        const v4u wg_86 = LDS128(lin[0], 1024); SB;
        WAITL(7); SB;
        MFMA(accG[3], wg_78, hf[12]); SB;
        const v4u wg_87 = LDS128(lin[0], 7168); SB;
        MFMA(accG[3], wg_79, hf[13]); SB;
        const v4u wg_88 = LDS128(lin[0], 2048); SB;
        WAITL(7); SB;
        MFMA(accG[4], wg_80, hf[12]); SB;
        const v4u wg_89 = LDS128(lin[0], 8192); SB;
        MFMA(accG[4], wg_81, hf[13]); SB;
        const v4u wg_90 = LDS128(lin[0], 3072); SB;
        WAITL(7); SB;
        MFMA(accG[5], wg_82, hf[12]); SB;
        const v4u wg_91 = LDS128(lin[0], 9216); SB;
        MFMA(accG[5], wg_83, hf[13]); SB;
        const v4u wg_92 = LDS128(lin[0], 4096); SB;
        WAITL(7); SB;
        MFMA(accG[0], wg_84, hf[14]); SB;
        const v4u wg_93 = LDS128(lin[0], 10240); SB;
        MFMA(accG[0], wg_85, hf[15]); SB;
        const v4u wg_94 = LDS128(lin[0], 5120); SB;
        WAITL(7); SB;
        MFMA(accG[1], wg_86, hf[14]); SB;
        const v4u wg_95 = LDS128(lin[0], 11264); SB;
        MFMA(accG[1], wg_87, hf[15]); SB;
        const v4u wg_96 = LDS128(lin[0], 12288); SB;
        WAITL(7); SB;
        MFMA(accG[2], wg_88, hf[14]); SB;
        const v4u wg_97 = LDS128(lin[0], 18432); SB;
        MFMA(accG[2], wg_89, hf[15]); SB;
        const v4u wg_98 = LDS128(lin[0], 13312); SB;
        WAITL(7); SB;
        MFMA(accG[3], wg_90, hf[14]); SB;
        const v4u wg_99 = LDS128(lin[0], 19456); SB;
        MFMA(accG[3], wg_91, hf[15]); SB;
        const v4u wg_100 = LDS128(lin[0], 14336); SB;
        WAITL(7); SB;
        MFMA(accG[4], wg_92, hf[14]); SB;
        const v4u wg_101 = LDS128(lin[0], 20480); SB;
        MFMA(accG[4], wg_93, hf[15]); SB;
        const v4u wg_102 = LDS128(lin[0], 15360); SB;
        WAITL(7); SB;
        MFMA(accG[5], wg_94, hf[14]); SB;
        const v4u wg_103 = LDS128(lin[0], 21504); SB;
        MFMA(accG[5], wg_95, hf[15]); SB;
        const v4u wg_104 = LDS128(lin[0], 16384); SB;
        WAITL(7); SB;
        MFMA(accG[0], wg_96, hf[16]); SB;
        const v4u wg_105 = LDS128(lin[0], 22528); SB;
        MFMA(accG[0], wg_97, hf[17]); SB;
        const v4u wg_106 = LDS128(lin[0], 17408); SB;
        WAITL(7); SB;
        MFMA(accG[1], wg_98, hf[16]); SB;
        const v4u wg_107 = LDS128(lin[0], 23552); SB;
        MFMA(accG[1], wg_99, hf[17]); SB;
        const v4u wg_108 = LDS128(lin[0], 24576); SB;
        WAITL(7); SB;
        MFMA(accG[2], wg_100, hf[16]); SB;
        const v4u wg_109 = LDS128(lin[0], 30720); SB;
        MFMA(accG[2], wg_101, hf[17]); SB;
        const v4u wg_110 = LDS128(lin[0], 25600); SB;
        WAITL(7); SB;
        MFMA(accG[3], wg_102, hf[16]); SB;
        const v4u wg_111 = LDS128(lin[0], 31744); SB;
        MFMA(accG[3], wg_103, hf[17]); SB;
        const v4u wg_112 = LDS128(lin[0], 26624); SB;
        WAITL(7); SB;
        MFMA(accG[4], wg_104, hf[16]); SB;
        const v4u wg_113 = LDS128(lin[0], 32768); SB;
        MFMA(accG[4], wg_105, hf[17]); SB;
        const v4u wg_114 = LDS128(lin[0], 27648); SB;
        WAITL(7); SB;
        MFMA(accG[5], wg_106, hf[16]); SB;
        const v4u wg_115 = LDS128(lin[0], 33792); SB;
        MFMA(accG[5], wg_107, hf[17]); SB;
        const v4u wg_116 = LDS128(lin[0], 28672); SB;
        WAITL(7); SB;
        MFMA(accG[0], wg_108, hf[18]); SB;
        const v4u wg_117 = LDS128(lin[0], 34816); SB;
        MFMA(accG[0], wg_109, hf[19]); SB;
        const v4u wg_118 = LDS128(lin[0], 29696); SB;
        WAITL(7); SB;
        MFMA(accG[1], wg_110, hf[18]); SB;
        const v4u wg_119 = LDS128(lin[0], 35840); SB;
        MFMA(accG[1], wg_111, hf[19]); SB;
        WAITL(6); SB;
        MFMA(accG[2], wg_112, hf[18]); SB;
        MFMA(accG[2], wg_113, hf[19]); SB;
        WAITL(4); SB;
        MFMA(accG[3], wg_114, hf[18]); SB;
        MFMA(accG[3], wg_115, hf[19]); SB;
        WAITL(2); SB;
        MFMA(accG[4], wg_116, hf[18]); SB;
        MFMA(accG[4], wg_117, hf[19]); SB;
        WAITL(0); SB;
        MFMA(accG[5], wg_118, hf[18]); SB;
        MFMA(accG[5], wg_119, hf[19]); SB;
        MFMA_END;
        SET_SWZ(w * 2048u); PRE_H();
        C_EPI_G();
        SKIP(201);
        NEW_LIN();
        BAR(); SB;
        const v4u ah_0_0_0 = LDS128(swz0[0], 61440); SB;
        const v4u ah_0_1_0 = LDS128(swz1[0], 61440); SB;
        const v4u bh_0_0_0 = LDS128(lin[0], 36864); SB;
        const v4u bh_0_1_0 = LDS128(lin[0], 43008); SB;
        const v4u bh_0_0_1 = LDS128(lin[0], 37888); SB;
        const v4u bh_0_1_1 = LDS128(lin[0], 44032); SB;
        const v4u bh_0_0_2 = LDS128(lin[0], 38912); SB;
        const v4u bh_0_1_2 = LDS128(lin[0], 45056); SB;
        WAITL(2); SB;
        MFMA(accH[0][0], bh_0_0_0, ah_0_0_0); SB;
        const v4u bh_0_0_3 = LDS128(lin[0], 39936); SB;
        MFMA(accH[0][0], bh_0_1_0, ah_0_1_0); SB;
        const v4u bh_0_1_3 = LDS128(lin[0], 46080); SB;
        const v4u bh_0_0_4 = LDS128(lin[0], 40960); SB;
        MFMA(accH[0][1], bh_0_0_1, ah_0_0_0); SB;
        const v4u bh_0_1_4 = LDS128(lin[0], 47104); SB;
        MFMA(accH[0][1], bh_0_1_1, ah_0_1_0); SB;
        const v4u bh_0_0_5 = LDS128(lin[0], 41984); SB;
        WAITL(3); SB;
        MFMA(accH[0][2], bh_0_0_2, ah_0_0_0); SB;
        const v4u bh_0_1_5 = LDS128(lin[0], 48128); SB;
        MFMA(accH[0][2], bh_0_1_2, ah_0_1_0); SB;
        BAR(); SB;
        const v4u ah_1_0_0 = LDS128(swz0[1], 4096); SB;
        MFMA(accH[0][3], bh_0_0_3, ah_0_0_0); SB;
        const v4u ah_1_1_0 = LDS128(swz1[1], 4096); SB;
        MFMA(accH[0][3], bh_0_1_3, ah_0_1_0); SB;
        const v4u bh_1_0_0 = LDS128(lin[2], 16384); SB;
        const v4u bh_1_1_0 = LDS128(lin[2], 22528); SB;
        WAITL(4); SB;
        MFMA(accH[0][4], bh_0_0_4, ah_0_0_0); SB;
        const v4u bh_1_0_1 = LDS128(lin[2], 17408); SB;
        MFMA(accH[0][4], bh_0_1_4, ah_0_1_0); SB;
        const v4u bh_1_1_1 = LDS128(lin[2], 23552); SB;
        MFMA(accH[0][5], bh_0_0_5, ah_0_0_0); SB;
        const v4u bh_1_0_2 = LDS128(lin[2], 18432); SB;
        MFMA(accH[0][5], bh_0_1_5, ah_0_1_0); SB;
        const v4u bh_1_1_2 = LDS128(lin[2], 24576); SB;
        WAITL(2); SB;
        MFMA(accH[0][0], bh_1_0_0, ah_1_0_0); SB;
        const v4u bh_1_0_3 = LDS128(lin[2], 19456); SB;
        MFMA(accH[0][0], bh_1_1_0, ah_1_1_0); SB;
        const v4u bh_1_1_3 = LDS128(lin[2], 25600); SB;
        const v4u bh_1_0_4 = LDS128(lin[2], 20480); SB;
        MFMA(accH[0][1], bh_1_0_1, ah_1_0_0); SB;
        const v4u bh_1_1_4 = LDS128(lin[2], 26624); SB;
        MFMA(accH[0][1], bh_1_1_1, ah_1_1_0); SB;
        const v4u bh_1_0_5 = LDS128(lin[2], 21504); SB;
        WAITL(3); SB;
        MFMA(accH[0][2], bh_1_0_2, ah_1_0_0); SB;
        const v4u bh_1_1_5 = LDS128(lin[2], 27648); SB;
        MFMA(accH[0][2], bh_1_1_2, ah_1_1_0); SB;
        BAR(); SB;
        const v4u bh_2_0_0 = LDS128(lin[0], 49152); SB;
        MFMA(accH[0][3], bh_1_0_3, ah_1_0_0); SB;
        const v4u bh_2_1_0 = LDS128(lin[0], 55296); SB;
        MFMA(accH[0][3], bh_1_1_3, ah_1_1_0); SB;
        const v4u bh_2_0_1 = LDS128(lin[0], 50176); SB;
        const v4u bh_2_1_1 = LDS128(lin[0], 56320); SB;
        WAITL(4); SB;
        MFMA(accH[0][4], bh_1_0_4, ah_1_0_0); SB;
        const v4u bh_2_0_2 = LDS128(lin[0], 51200); SB;
        MFMA(accH[0][4], bh_1_1_4, ah_1_1_0); SB;
        const v4u bh_2_1_2 = LDS128(lin[0], 57344); SB;
        MFMA(accH[0][5], bh_1_0_5, ah_1_0_0); SB;
        const v4u bh_2_0_3 = LDS128(lin[0], 52224); SB;
        MFMA(accH[0][5], bh_1_1_5, ah_1_1_0); SB;
        const v4u bh_2_1_3 = LDS128(lin[0], 58368); SB;
        WAITL(4); SB;
        MFMA(accH[0][0], bh_2_0_0, sa[0]); SB;
        const v4u bh_2_0_4 = LDS128(lin[0], 53248); SB;
        MFMA(accH[0][0], bh_2_1_0, sa[1]); SB;
        const v4u bh_2_1_4 = LDS128(lin[0], 59392); SB;
        MFMA(accH[0][1], bh_2_0_1, sa[0]); SB;
        const v4u bh_2_0_5 = LDS128(lin[0], 54272); SB;
        MFMA(accH[0][1], bh_2_1_1, sa[1]); SB;
        const v4u bh_2_1_5 = LDS128(lin[0], 60416); SB;
        WAITL(4); SB;
        MFMA(accH[0][2], bh_2_0_2, sa[0]); SB;
        BAR(); SB;
        const v4u ah_3_0_0 = LDS128(swz0[1], 12288); SB;
        MFMA(accH[0][2], bh_2_1_2, sa[1]); SB;
        const v4u ah_3_1_0 = LDS128(swz1[1], 12288); SB;
        MFMA(accH[0][3], bh_2_0_3, sa[0]); SB;
        const v4u bh_3_0_0 = LDS128(lin[0], 0); SB;
        MFMA(accH[0][3], bh_2_1_3, sa[1]); SB;
        const v4u bh_3_1_0 = LDS128(lin[0], 6144); SB;
        WAITL(4); SB;
        MFMA(accH[0][4], bh_2_0_4, sa[0]); SB;
        const v4u bh_3_0_1 = LDS128(lin[0], 1024); SB;
        MFMA(accH[0][4], bh_2_1_4, sa[1]); SB;
        const v4u bh_3_1_1 = LDS128(lin[0], 7168); SB;
        MFMA(accH[0][5], bh_2_0_5, sa[0]); SB;
        const v4u bh_3_0_2 = LDS128(lin[0], 2048); SB;
        MFMA(accH[0][5], bh_2_1_5, sa[1]); SB;
        const v4u bh_3_1_2 = LDS128(lin[0], 8192); SB;
        WAITL(2); SB;
        MFMA(accH[0][0], bh_3_0_0, ah_3_0_0); SB;
        const v4u bh_3_0_3 = LDS128(lin[0], 3072); SB;
        MFMA(accH[0][0], bh_3_1_0, ah_3_1_0); SB;
        const v4u bh_3_1_3 = LDS128(lin[0], 9216); SB;
        const v4u bh_3_0_4 = LDS128(lin[0], 4096); SB;
        MFMA(accH[0][1], bh_3_0_1, ah_3_0_0); SB;
        const v4u bh_3_1_4 = LDS128(lin[0], 10240); SB;
        MFMA(accH[0][1], bh_3_1_1, ah_3_1_0); SB;
        const v4u bh_3_0_5 = LDS128(lin[0], 5120); SB;
        WAITL(3); SB;
        MFMA(accH[0][2], bh_3_0_2, ah_3_0_0); SB;
        const v4u bh_3_1_5 = LDS128(lin[0], 11264); SB;
        MFMA(accH[0][2], bh_3_1_2, ah_3_1_0); SB;
        BAR(); SB;
        const v4u ah_4_0_0 = LDS128(swz0[1], 20480); SB;
        MFMA(accH[0][3], bh_3_0_3, ah_3_0_0); SB;
        const v4u ah_4_1_0 = LDS128(swz1[1], 20480); SB;
        MFMA(accH[0][3], bh_3_1_3, ah_3_1_0); SB;
        const v4u bh_4_0_0 = LDS128(lin[0], 12288); SB;
        const v4u bh_4_1_0 = LDS128(lin[0], 18432); SB;
        WAITL(4); SB;
        MFMA(accH[0][4], bh_3_0_4, ah_3_0_0); SB;
        const v4u bh_4_0_1 = LDS128(lin[0], 13312); SB;
        MFMA(accH[0][4], bh_3_1_4, ah_3_1_0); SB;
        const v4u bh_4_1_1 = LDS128(lin[0], 19456); SB;
        MFMA(accH[0][5], bh_3_0_5, ah_3_0_0); SB;
        const v4u bh_4_0_2 = LDS128(lin[0], 14336); SB;
        MFMA(accH[0][5], bh_3_1_5, ah_3_1_0); SB;
        const v4u bh_4_1_2 = LDS128(lin[0], 20480); SB;
        WAITL(2); SB;
        MFMA(accH[0][0], bh_4_0_0, ah_4_0_0); SB;
        const v4u bh_4_0_3 = LDS128(lin[0], 15360); SB;
        MFMA(accH[0][0], bh_4_1_0, ah_4_1_0); SB;
        const v4u bh_4_1_3 = LDS128(lin[0], 21504); SB;
        const v4u bh_4_0_4 = LDS128(lin[0], 16384); SB;
        MFMA(accH[0][1], bh_4_0_1, ah_4_0_0); SB;
        const v4u bh_4_1_4 = LDS128(lin[0], 22528); SB;
        MFMA(accH[0][1], bh_4_1_1, ah_4_1_0); SB;
        const v4u bh_4_0_5 = LDS128(lin[0], 17408); SB;
        WAITL(3); SB;
        MFMA(accH[0][2], bh_4_0_2, ah_4_0_0); SB;
        const v4u bh_4_1_5 = LDS128(lin[0], 23552); SB;
        MFMA(accH[0][2], bh_4_1_2, ah_4_1_0); SB;
        BAR(); SB;
        const v4u ah_5_0_0 = LDS128(swz0[1], 28672); SB;
        MFMA(accH[0][3], bh_4_0_3, ah_4_0_0); SB;
        const v4u ah_5_1_0 = LDS128(swz1[1], 28672); SB;
        MFMA(accH[0][3], bh_4_1_3, ah_4_1_0); SB;
        const v4u bh_5_0_0 = LDS128(lin[0], 24576); SB;
        const v4u bh_5_1_0 = LDS128(lin[0], 30720); SB;
        WAITL(4); SB;
        MFMA(accH[0][4], bh_4_0_4, ah_4_0_0); SB;
        const v4u bh_5_0_1 = LDS128(lin[0], 25600); SB;
        MFMA(accH[0][4], bh_4_1_4, ah_4_1_0); SB;
        const v4u bh_5_1_1 = LDS128(lin[0], 31744); SB;
        MFMA(accH[0][5], bh_4_0_5, ah_4_0_0); SB;
        const v4u bh_5_0_2 = LDS128(lin[0], 26624); SB;
        MFMA(accH[0][5], bh_4_1_5, ah_4_1_0); SB;
        const v4u bh_5_1_2 = LDS128(lin[0], 32768); SB;
        WAITL(2); SB;
        MFMA(accH[0][0], bh_5_0_0, ah_5_0_0); SB;
        const v4u bh_5_0_3 = LDS128(lin[0], 27648); SB;
        MFMA(accH[0][0], bh_5_1_0, ah_5_1_0); SB;
        const v4u bh_5_1_3 = LDS128(lin[0], 33792); SB;
        const v4u bh_5_0_4 = LDS128(lin[0], 28672); SB;
        MFMA(accH[0][1], bh_5_0_1, ah_5_0_0); SB;
        const v4u bh_5_1_4 = LDS128(lin[0], 34816); SB;
        MFMA(accH[0][1], bh_5_1_1, ah_5_1_0); SB;
        const v4u bh_5_0_5 = LDS128(lin[0], 29696); SB;
        WAITL(3); SB;
        MFMA(accH[0][2], bh_5_0_2, ah_5_0_0); SB;
        const v4u bh_5_1_5 = LDS128(lin[0], 35840); SB;
        MFMA(accH[0][2], bh_5_1_2, ah_5_1_0); SB;
        BAR(); SB;
        const v4u bh_6_0_0 = LDS128(lin[0], 36864); SB;
        MFMA(accH[0][3], bh_5_0_3, ah_5_0_0); SB;
        const v4u bh_6_1_0 = LDS128(lin[0], 43008); SB;
        MFMA(accH[0][3], bh_5_1_3, ah_5_1_0); SB;
        const v4u bh_6_0_1 = LDS128(lin[0], 37888); SB;
        const v4u bh_6_1_1 = LDS128(lin[0], 44032); SB;
        WAITL(4); SB;
        MFMA(accH[0][4], bh_5_0_4, ah_5_0_0); SB;
        const v4u bh_6_0_2 = LDS128(lin[0], 38912); SB;
        MFMA(accH[0][4], bh_5_1_4, ah_5_1_0); SB;
        const v4u bh_6_1_2 = LDS128(lin[0], 45056); SB;
        MFMA(accH[0][5], bh_5_0_5, ah_5_0_0); SB;
        const v4u bh_6_0_3 = LDS128(lin[0], 39936); SB;
        MFMA(accH[0][5], bh_5_1_5, ah_5_1_0); SB;
        const v4u bh_6_1_3 = LDS128(lin[0], 46080); SB;
        WAITL(4); SB;
        MFMA(accH[0][0], bh_6_0_0, sa[2]); SB;
        const v4u bh_6_0_4 = LDS128(lin[0], 40960); SB;
        MFMA(accH[0][0], bh_6_1_0, sa[3]); SB;
        const v4u bh_6_1_4 = LDS128(lin[0], 47104); SB;
        MFMA(accH[0][1], bh_6_0_1, sa[2]); SB;
        const v4u bh_6_0_5 = LDS128(lin[0], 41984); SB;
        MFMA(accH[0][1], bh_6_1_1, sa[3]); SB;
        const v4u bh_6_1_5 = LDS128(lin[0], 48128); SB;
        WAITL(4); SB;
        MFMA(accH[0][2], bh_6_0_2, sa[2]); SB;
        BAR(); SB;
        const v4u ah_7_0_0 = LDS128(swz0[1], 36864); SB;
        MFMA(accH[0][2], bh_6_1_2, sa[3]); SB;
        const v4u ah_7_1_0 = LDS128(swz1[1], 36864); SB;
        MFMA(accH[0][3], bh_6_0_3, sa[2]); SB;
        const v4u bh_7_0_0 = LDS128(lin[2], 16384); SB;
        MFMA(accH[0][3], bh_6_1_3, sa[3]); SB;
        const v4u bh_7_1_0 = LDS128(lin[2], 22528); SB;
        WAITL(4); SB;
        MFMA(accH[0][4], bh_6_0_4, sa[2]); SB;
        const v4u bh_7_0_1 = LDS128(lin[2], 17408); SB;
        MFMA(accH[0][4], bh_6_1_4, sa[3]); SB;
        const v4u bh_7_1_1 = LDS128(lin[2], 23552); SB;
        MFMA(accH[0][5], bh_6_0_5, sa[2]); SB;
        const v4u bh_7_0_2 = LDS128(lin[2], 18432); SB;
        MFMA(accH[0][5], bh_6_1_5, sa[3]); SB;
        const v4u bh_7_1_2 = LDS128(lin[2], 24576); SB;
        WAITL(2); SB;
        MFMA(accH[0][0], bh_7_0_0, ah_7_0_0); SB;
        const v4u bh_7_0_3 = LDS128(lin[2], 19456); SB;
        MFMA(accH[0][0], bh_7_1_0, ah_7_1_0); SB;
        const v4u bh_7_1_3 = LDS128(lin[2], 25600); SB;
        const v4u bh_7_0_4 = LDS128(lin[2], 20480); SB;
        MFMA(accH[0][1], bh_7_0_1, ah_7_0_0); SB;
        const v4u bh_7_1_4 = LDS128(lin[2], 26624); SB;
        MFMA(accH[0][1], bh_7_1_1, ah_7_1_0); SB;
        const v4u bh_7_0_5 = LDS128(lin[2], 21504); SB;
        WAITL(3); SB;
        MFMA(accH[0][2], bh_7_0_2, ah_7_0_0); SB;
        const v4u bh_7_1_5 = LDS128(lin[2], 27648); SB;
        MFMA(accH[0][2], bh_7_1_2, ah_7_1_0); SB;
        BAR(); SB;
        const v4u ah_8_0_0 = LDS128(swz0[1], 45056); SB;
        MFMA(accH[0][3], bh_7_0_3, ah_7_0_0); SB;
        const v4u ah_8_1_0 = LDS128(swz1[1], 45056); SB;
        MFMA(accH[0][3], bh_7_1_3, ah_7_1_0); SB;
        const v4u bh_8_0_0 = LDS128(lin[0], 49152); SB;
        const v4u bh_8_1_0 = LDS128(lin[0], 55296); SB;
        WAITL(4); SB;
        MFMA(accH[0][4], bh_7_0_4, ah_7_0_0); SB;
        const v4u bh_8_0_1 = LDS128(lin[0], 50176); SB;
        MFMA(accH[0][4], bh_7_1_4, ah_7_1_0); SB;
        const v4u bh_8_1_1 = LDS128(lin[0], 56320); SB;
        MFMA(accH[0][5], bh_7_0_5, ah_7_0_0); SB;
        const v4u bh_8_0_2 = LDS128(lin[0], 51200); SB;
        MFMA(accH[0][5], bh_7_1_5, ah_7_1_0); SB;
        const v4u bh_8_1_2 = LDS128(lin[0], 57344); SB;
        WAITL(2); SB;
        MFMA(accH[0][0], bh_8_0_0, ah_8_0_0); SB;
        const v4u bh_8_0_3 = LDS128(lin[0], 52224); SB;
        MFMA(accH[0][0], bh_8_1_0, ah_8_1_0); SB;
        const v4u bh_8_1_3 = LDS128(lin[0], 58368); SB;
        const v4u bh_8_0_4 = LDS128(lin[0], 53248); SB;
        MFMA(accH[0][1], bh_8_0_1, ah_8_0_0); SB;
        const v4u bh_8_1_4 = LDS128(lin[0], 59392); SB;
        MFMA(accH[0][1], bh_8_1_1, ah_8_1_0); SB;
        const v4u bh_8_0_5 = LDS128(lin[0], 54272); SB;
        WAITL(3); SB;
        MFMA(accH[0][2], bh_8_0_2, ah_8_0_0); SB;
        const v4u bh_8_1_5 = LDS128(lin[0], 60416); SB;
        MFMA(accH[0][2], bh_8_1_2, ah_8_1_0); SB;
        BAR(); SB;
        const v4u ah_9_0_0 = LDS128(swz0[1], 53248); SB;
        MFMA(accH[0][3], bh_8_0_3, ah_8_0_0); SB;
        const v4u ah_9_1_0 = LDS128(swz1[1], 53248); SB;
        MFMA(accH[0][3], bh_8_1_3, ah_8_1_0); SB;
        const v4u bh_9_0_0 = LDS128(lin[0], 0); SB;
        const v4u bh_9_1_0 = LDS128(lin[0], 6144); SB;
        WAITL(4); SB;
        MFMA(accH[0][4], bh_8_0_4, ah_8_0_0); SB;
        const v4u bh_9_0_1 = LDS128(lin[0], 1024); SB;
        MFMA(accH[0][4], bh_8_1_4, ah_8_1_0); SB;
        const v4u bh_9_1_1 = LDS128(lin[0], 7168); SB;
        MFMA(accH[0][5], bh_8_0_5, ah_8_0_0); SB;
        const v4u bh_9_0_2 = LDS128(lin[0], 2048); SB;
        MFMA(accH[0][5], bh_8_1_5, ah_8_1_0); SB;
        const v4u bh_9_1_2 = LDS128(lin[0], 8192); SB;
        WAITL(2); SB;
        MFMA(accH[0][0], bh_9_0_0, ah_9_0_0); SB;
        const v4u bh_9_0_3 = LDS128(lin[0], 3072); SB;
        MFMA(accH[0][0], bh_9_1_0, ah_9_1_0); SB;
        const v4u bh_9_1_3 = LDS128(lin[0], 9216); SB;
        const v4u bh_9_0_4 = LDS128(lin[0], 4096); SB;
        MFMA(accH[0][1], bh_9_0_1, ah_9_0_0); SB;
        const v4u bh_9_1_4 = LDS128(lin[0], 10240); SB;
        MFMA(accH[0][1], bh_9_1_1, ah_9_1_0); SB;
        const v4u bh_9_0_5 = LDS128(lin[0], 5120); SB;
        WAITL(3); SB;
        MFMA(accH[0][2], bh_9_0_2, ah_9_0_0); SB;
        const v4u bh_9_1_5 = LDS128(lin[0], 11264); SB;
        MFMA(accH[0][2], bh_9_1_2, ah_9_1_0); SB;
        BAR(); SB;
        const v4u ah_10_0_0 = LDS128(swz0[1], 61440); SB;
        MFMA(accH[0][3], bh_9_0_3, ah_9_0_0); SB;
        const v4u ah_10_1_0 = LDS128(swz1[1], 61440); SB;
        MFMA(accH[0][3], bh_9_1_3, ah_9_1_0); SB;
        const v4u bh_10_0_0 = LDS128(lin[0], 12288); SB;
        const v4u bh_10_1_0 = LDS128(lin[0], 18432); SB;
        WAITL(4); SB;
        MFMA(accH[0][4], bh_9_0_4, ah_9_0_0); SB;
        const v4u bh_10_0_1 = LDS128(lin[0], 13312); SB;
        MFMA(accH[0][4], bh_9_1_4, ah_9_1_0); SB;
        const v4u bh_10_1_1 = LDS128(lin[0], 19456); SB;
        MFMA(accH[0][5], bh_9_0_5, ah_9_0_0); SB;
        const v4u bh_10_0_2 = LDS128(lin[0], 14336); SB;
        MFMA(accH[0][5], bh_9_1_5, ah_9_1_0); SB;
        const v4u bh_10_1_2 = LDS128(lin[0], 20480); SB;
        WAITL(2); SB;
        MFMA(accH[0][0], bh_10_0_0, ah_10_0_0); SB;
        const v4u bh_10_0_3 = LDS128(lin[0], 15360); SB;
        MFMA(accH[0][0], bh_10_1_0, ah_10_1_0); SB;
        const v4u bh_10_1_3 = LDS128(lin[0], 21504); SB;
        const v4u bh_10_0_4 = LDS128(lin[0], 16384); SB;
        MFMA(accH[0][1], bh_10_0_1, ah_10_0_0); SB;
        const v4u bh_10_1_4 = LDS128(lin[0], 22528); SB;
        MFMA(accH[0][1], bh_10_1_1, ah_10_1_0); SB;
        const v4u bh_10_0_5 = LDS128(lin[0], 17408); SB;
        WAITL(3); SB;
        MFMA(accH[0][2], bh_10_0_2, ah_10_0_0); SB;
        const v4u bh_10_1_5 = LDS128(lin[0], 23552); SB;
        MFMA(accH[0][2], bh_10_1_2, ah_10_1_0); SB;
        BAR(); SB;
        const v4u bh_11_0_0 = LDS128(lin[0], 24576); SB;
        MFMA(accH[0][3], bh_10_0_3, ah_10_0_0); SB;
        const v4u bh_11_1_0 = LDS128(lin[0], 30720); SB;
        MFMA(accH[0][3], bh_10_1_3, ah_10_1_0); SB;
        const v4u bh_11_0_1 = LDS128(lin[0], 25600); SB;
        const v4u bh_11_1_1 = LDS128(lin[0], 31744); SB;
        WAITL(4); SB;
        MFMA(accH[0][4], bh_10_0_4, ah_10_0_0); SB;
        const v4u bh_11_0_2 = LDS128(lin[0], 26624); SB;
        MFMA(accH[0][4], bh_10_1_4, ah_10_1_0); SB;
        const v4u bh_11_1_2 = LDS128(lin[0], 32768); SB;
        MFMA(accH[0][5], bh_10_0_5, ah_10_0_0); SB;
        const v4u bh_11_0_3 = LDS128(lin[0], 27648); SB;
        MFMA(accH[0][5], bh_10_1_5, ah_10_1_0); SB;
        const v4u bh_11_1_3 = LDS128(lin[0], 33792); SB;
        WAITL(4); SB;
        MFMA(accH[0][0], bh_11_0_0, sa[4]); SB;
        const v4u bh_11_0_4 = LDS128(lin[0], 28672); SB;
        MFMA(accH[0][0], bh_11_1_0, sa[5]); SB;
        const v4u bh_11_1_4 = LDS128(lin[0], 34816); SB;
        MFMA(accH[0][1], bh_11_0_1, sa[4]); SB;
        const v4u bh_11_0_5 = LDS128(lin[0], 29696); SB;
        MFMA(accH[0][1], bh_11_1_1, sa[5]); SB;
        const v4u bh_11_1_5 = LDS128(lin[0], 35840); SB;
        WAITL(4); SB;
        MFMA(accH[0][2], bh_11_0_2, sa[4]); SB;
        BAR(); SB;
        const v4u ah_12_0_0 = LDS128(swz0[2], 4096); SB;
        MFMA(accH[0][2], bh_11_1_2, sa[5]); SB;
        const v4u ah_12_1_0 = LDS128(swz1[2], 4096); SB;
        MFMA(accH[0][3], bh_11_0_3, sa[4]); SB;
        const v4u bh_12_0_0 = LDS128(lin[0], 36864); SB;
        MFMA(accH[0][3], bh_11_1_3, sa[5]); SB;
        const v4u bh_12_1_0 = LDS128(lin[0], 43008); SB;
        WAITL(4); SB;
        MFMA(accH[0][4], bh_11_0_4, sa[4]); SB;
        const v4u bh_12_0_1 = LDS128(lin[0], 37888); SB;
        MFMA(accH[0][4], bh_11_1_4, sa[5]); SB;
        const v4u bh_12_1_1 = LDS128(lin[0], 44032); SB;
        MFMA(accH[0][5], bh_11_0_5, sa[4]); SB;
        const v4u bh_12_0_2 = LDS128(lin[0], 38912); SB;
        MFMA(accH[0][5], bh_11_1_5, sa[5]); SB;
        const v4u bh_12_1_2 = LDS128(lin[0], 45056); SB;
        WAITL(2); SB;
        MFMA(accH[0][0], bh_12_0_0, ah_12_0_0); SB;
        const v4u bh_12_0_3 = LDS128(lin[0], 39936); SB;
        MFMA(accH[0][0], bh_12_1_0, ah_12_1_0); SB;
        const v4u bh_12_1_3 = LDS128(lin[0], 46080); SB;
        const v4u bh_12_0_4 = LDS128(lin[0], 40960); SB;
        MFMA(accH[0][1], bh_12_0_1, ah_12_0_0); SB;
        const v4u bh_12_1_4 = LDS128(lin[0], 47104); SB;
        MFMA(accH[0][1], bh_12_1_1, ah_12_1_0); SB;
        const v4u bh_12_0_5 = LDS128(lin[0], 41984); SB;
        WAITL(3); SB;
        MFMA(accH[0][2], bh_12_0_2, ah_12_0_0); SB;
        const v4u bh_12_1_5 = LDS128(lin[0], 48128); SB;
        MFMA(accH[0][2], bh_12_1_2, ah_12_1_0); SB;
        BAR(); SB;
        const v4u ah_13_0_0 = LDS128(swz0[0], 61440); SB;
        MFMA(accH[0][3], bh_12_0_3, ah_12_0_0); SB;
        const v4u ah_13_1_0 = LDS128(swz1[0], 61440); SB;
        MFMA(accH[0][3], bh_12_1_3, ah_12_1_0); SB;
        const v4u bh_13_0_0 = LDS128(lin[2], 16384); SB;
        const v4u bh_13_1_0 = LDS128(lin[2], 22528); SB;
        WAITL(4); SB;
        MFMA(accH[0][4], bh_12_0_4, ah_12_0_0); SB;
        const v4u bh_13_0_1 = LDS128(lin[2], 17408); SB;
        MFMA(accH[0][4], bh_12_1_4, ah_12_1_0); SB;
        const v4u bh_13_1_1 = LDS128(lin[2], 23552); SB;
        MFMA(accH[0][5], bh_12_0_5, ah_12_0_0); SB;
        const v4u bh_13_0_2 = LDS128(lin[2], 18432); SB;
        MFMA(accH[0][5], bh_12_1_5, ah_12_1_0); SB;
        const v4u bh_13_1_2 = LDS128(lin[2], 24576); SB;
        WAITL(2); SB;
        MFMA(accH[0][0], bh_13_0_0, ah_13_0_0); SB;
        const v4u bh_13_0_3 = LDS128(lin[2], 19456); SB;
        MFMA(accH[0][0], bh_13_1_0, ah_13_1_0); SB;
        const v4u bh_13_1_3 = LDS128(lin[2], 25600); SB;
        const v4u bh_13_0_4 = LDS128(lin[2], 20480); SB;
        MFMA(accH[0][1], bh_13_0_1, ah_13_0_0); SB;
        const v4u bh_13_1_4 = LDS128(lin[2], 26624); SB;
        MFMA(accH[0][1], bh_13_1_1, ah_13_1_0); SB;
        const v4u bh_13_0_5 = LDS128(lin[2], 21504); SB;
        WAITL(3); SB;
        MFMA(accH[0][2], bh_13_0_2, ah_13_0_0); SB;
        const v4u bh_13_1_5 = LDS128(lin[2], 27648); SB;
        MFMA(accH[0][2], bh_13_1_2, ah_13_1_0); SB;
        BAR(); SB;
        const v4u ah_14_0_0 = LDS128(swz0[1], 4096); SB;
        MFMA(accH[0][3], bh_13_0_3, ah_13_0_0); SB;
        const v4u ah_14_1_0 = LDS128(swz1[1], 4096); SB;
        MFMA(accH[0][3], bh_13_1_3, ah_13_1_0); SB;
        const v4u bh_14_0_0 = LDS128(lin[0], 49152); SB;
        const v4u bh_14_1_0 = LDS128(lin[0], 55296); SB;
        WAITL(4); SB;
        MFMA(accH[0][4], bh_13_0_4, ah_13_0_0); SB;
        const v4u bh_14_0_1 = LDS128(lin[0], 50176); SB;
        MFMA(accH[0][4], bh_13_1_4, ah_13_1_0); SB;
        const v4u bh_14_1_1 = LDS128(lin[0], 56320); SB;
        MFMA(accH[0][5], bh_13_0_5, ah_13_0_0); SB;
        const v4u bh_14_0_2 = LDS128(lin[0], 51200); SB;
        MFMA(accH[0][5], bh_13_1_5, ah_13_1_0); SB;
        const v4u bh_14_1_2 = LDS128(lin[0], 57344); SB;
        WAITL(2); SB;
        MFMA(accH[0][0], bh_14_0_0, ah_14_0_0); SB;
        const v4u bh_14_0_3 = LDS128(lin[0], 52224); SB;
        MFMA(accH[0][0], bh_14_1_0, ah_14_1_0); SB;
        const v4u bh_14_1_3 = LDS128(lin[0], 58368); SB;
        const v4u bh_14_0_4 = LDS128(lin[0], 53248); SB;
        MFMA(accH[0][1], bh_14_0_1, ah_14_0_0); SB;
        const v4u bh_14_1_4 = LDS128(lin[0], 59392); SB;
        MFMA(accH[0][1], bh_14_1_1, ah_14_1_0); SB;
        const v4u bh_14_0_5 = LDS128(lin[0], 54272); SB;
        WAITL(3); SB;
        MFMA(accH[0][2], bh_14_0_2, ah_14_0_0); SB;
        const v4u bh_14_1_5 = LDS128(lin[0], 60416); SB;
        MFMA(accH[0][2], bh_14_1_2, ah_14_1_0); SB;
        BAR(); SB;
        const v4u ah_15_0_0 = LDS128(swz0[1], 12288); SB;
        MFMA(accH[0][3], bh_14_0_3, ah_14_0_0); SB;
        const v4u ah_15_1_0 = LDS128(swz1[1], 12288); SB;
        MFMA(accH[0][3], bh_14_1_3, ah_14_1_0); SB;
        const v4u bh_15_0_0 = LDS128(lin[0], 0); SB;
        const v4u bh_15_1_0 = LDS128(lin[0], 6144); SB;
        WAITL(4); SB;
        MFMA(accH[0][4], bh_14_0_4, ah_14_0_0); SB;
        const v4u bh_15_0_1 = LDS128(lin[0], 1024); SB;
        MFMA(accH[0][4], bh_14_1_4, ah_14_1_0); SB;
        const v4u bh_15_1_1 = LDS128(lin[0], 7168); SB;
        MFMA(accH[0][5], bh_14_0_5, ah_14_0_0); SB;
        const v4u bh_15_0_2 = LDS128(lin[0], 2048); SB;
        MFMA(accH[0][5], bh_14_1_5, ah_14_1_0); SB;
        const v4u bh_15_1_2 = LDS128(lin[0], 8192); SB;
        WAITL(2); SB;
        MFMA(accH[0][0], bh_15_0_0, ah_15_0_0); SB;
        const v4u bh_15_0_3 = LDS128(lin[0], 3072); SB;
        MFMA(accH[0][0], bh_15_1_0, ah_15_1_0); SB;
        const v4u bh_15_1_3 = LDS128(lin[0], 9216); SB;
        const v4u bh_15_0_4 = LDS128(lin[0], 4096); SB;
        MFMA(accH[0][1], bh_15_0_1, ah_15_0_0); SB;
        const v4u bh_15_1_4 = LDS128(lin[0], 10240); SB;
        MFMA(accH[0][1], bh_15_1_1, ah_15_1_0); SB;
        const v4u bh_15_0_5 = LDS128(lin[0], 5120); SB;
        WAITL(3); SB;
        MFMA(accH[0][2], bh_15_0_2, ah_15_0_0); SB;
        const v4u bh_15_1_5 = LDS128(lin[0], 11264); SB;
        MFMA(accH[0][2], bh_15_1_2, ah_15_1_0); SB;
        BAR(); SB;
        const v4u bh_16_0_0 = LDS128(lin[0], 12288); SB;
        MFMA(accH[0][3], bh_15_0_3, ah_15_0_0); SB;
        const v4u bh_16_1_0 = LDS128(lin[0], 18432); SB;
        MFMA(accH[0][3], bh_15_1_3, ah_15_1_0); SB;
        const v4u bh_16_0_1 = LDS128(lin[0], 13312); SB;
        const v4u bh_16_1_1 = LDS128(lin[0], 19456); SB;
        WAITL(4); SB;
        MFMA(accH[0][4], bh_15_0_4, ah_15_0_0); SB;
        const v4u bh_16_0_2 = LDS128(lin[0], 14336); SB;
        MFMA(accH[0][4], bh_15_1_4, ah_15_1_0); SB;
        const v4u bh_16_1_2 = LDS128(lin[0], 20480); SB;
        MFMA(accH[0][5], bh_15_0_5, ah_15_0_0); SB;
        const v4u bh_16_0_3 = LDS128(lin[0], 15360); SB;
        MFMA(accH[0][5], bh_15_1_5, ah_15_1_0); SB;
        const v4u bh_16_1_3 = LDS128(lin[0], 21504); SB;
        WAITL(4); SB;
        MFMA(accH[0][0], bh_16_0_0, sa[6]); SB;
        const v4u bh_16_0_4 = LDS128(lin[0], 16384); SB;
        MFMA(accH[0][0], bh_16_1_0, sa[7]); SB;
        const v4u bh_16_1_4 = LDS128(lin[0], 22528); SB;
        MFMA(accH[0][1], bh_16_0_1, sa[6]); SB;
        const v4u bh_16_0_5 = LDS128(lin[0], 17408); SB;
        MFMA(accH[0][1], bh_16_1_1, sa[7]); SB;
        const v4u bh_16_1_5 = LDS128(lin[0], 23552); SB;
        WAITL(4); SB;
        MFMA(accH[0][2], bh_16_0_2, sa[6]); SB;
        BAR(); SB;
        const v4u ah_17_0_0 = LDS128(swz0[1], 20480); SB;
        MFMA(accH[0][2], bh_16_1_2, sa[7]); SB;
        const v4u ah_17_1_0 = LDS128(swz1[1], 20480); SB;
        MFMA(accH[0][3], bh_16_0_3, sa[6]); SB;
        const v4u bh_17_0_0 = LDS128(lin[0], 24576); SB;
        MFMA(accH[0][3], bh_16_1_3, sa[7]); SB;
        const v4u bh_17_1_0 = LDS128(lin[0], 30720); SB;
        WAITL(4); SB;
        MFMA(accH[0][4], bh_16_0_4, sa[6]); SB;
        const v4u bh_17_0_1 = LDS128(lin[0], 25600); SB;
        MFMA(accH[0][4], bh_16_1_4, sa[7]); SB;
        const v4u bh_17_1_1 = LDS128(lin[0], 31744); SB;
        MFMA(accH[0][5], bh_16_0_5, sa[6]); SB;
        const v4u bh_17_0_2 = LDS128(lin[0], 26624); SB;
        MFMA(accH[0][5], bh_16_1_5, sa[7]); SB;
        const v4u bh_17_1_2 = LDS128(lin[0], 32768); SB;
        WAITL(2); SB;
        MFMA(accH[0][0], bh_17_0_0, ah_17_0_0); SB;
        const v4u bh_17_0_3 = LDS128(lin[0], 27648); SB;
        MFMA(accH[0][0], bh_17_1_0, ah_17_1_0); SB;
        const v4u bh_17_1_3 = LDS128(lin[0], 33792); SB;
        const v4u bh_17_0_4 = LDS128(lin[0], 28672); SB;
        MFMA(accH[0][1], bh_17_0_1, ah_17_0_0); SB;
        const v4u bh_17_1_4 = LDS128(lin[0], 34816); SB;
        MFMA(accH[0][1], bh_17_1_1, ah_17_1_0); SB;
        const v4u bh_17_0_5 = LDS128(lin[0], 29696); SB;
        WAITL(3); SB;
        MFMA(accH[0][2], bh_17_0_2, ah_17_0_0); SB;
        const v4u bh_17_1_5 = LDS128(lin[0], 35840); SB;
        MFMA(accH[0][2], bh_17_1_2, ah_17_1_0); SB;
        BAR(); SB;
        const v4u ah_18_0_0 = LDS128(swz0[1], 28672); SB;
        MFMA(accH[0][3], bh_17_0_3, ah_17_0_0); SB;
        const v4u ah_18_1_0 = LDS128(swz1[1], 28672); SB;
        MFMA(accH[0][3], bh_17_1_3, ah_17_1_0); SB;
        const v4u bh_18_0_0 = LDS128(lin[0], 36864); SB;
        const v4u bh_18_1_0 = LDS128(lin[0], 43008); SB;
        WAITL(4); SB;
        MFMA(accH[0][4], bh_17_0_4, ah_17_0_0); SB;
        const v4u bh_18_0_1 = LDS128(lin[0], 37888); SB;
        MFMA(accH[0][4], bh_17_1_4, ah_17_1_0); SB;
        const v4u bh_18_1_1 = LDS128(lin[0], 44032); SB;
        MFMA(accH[0][5], bh_17_0_5, ah_17_0_0); SB;
        const v4u bh_18_0_2 = LDS128(lin[0], 38912); SB;
        MFMA(accH[0][5], bh_17_1_5, ah_17_1_0); SB;
        const v4u bh_18_1_2 = LDS128(lin[0], 45056); SB;
        WAITL(2); SB;
        MFMA(accH[0][0], bh_18_0_0, ah_18_0_0); SB;
        const v4u bh_18_0_3 = LDS128(lin[0], 39936); SB;
        MFMA(accH[0][0], bh_18_1_0, ah_18_1_0); SB;
        const v4u bh_18_1_3 = LDS128(lin[0], 46080); SB;
        const v4u bh_18_0_4 = LDS128(lin[0], 40960); SB;
        MFMA(accH[0][1], bh_18_0_1, ah_18_0_0); SB;
        const v4u bh_18_1_4 = LDS128(lin[0], 47104); SB;
        MFMA(accH[0][1], bh_18_1_1, ah_18_1_0); SB;
        const v4u bh_18_0_5 = LDS128(lin[0], 41984); SB;
        WAITL(3); SB;
        MFMA(accH[0][2], bh_18_0_2, ah_18_0_0); SB;
        const v4u bh_18_1_5 = LDS128(lin[0], 48128); SB;
        MFMA(accH[0][2], bh_18_1_2, ah_18_1_0); SB;
        BAR(); SB;
        const v4u ah_19_0_0 = LDS128(swz0[1], 36864); SB;
        MFMA(accH[0][3], bh_18_0_3, ah_18_0_0); SB;
        const v4u ah_19_1_0 = LDS128(swz1[1], 36864); SB;
        MFMA(accH[0][3], bh_18_1_3, ah_18_1_0); SB;
        const v4u bh_19_0_0 = LDS128(lin[2], 16384); SB;
        const v4u bh_19_1_0 = LDS128(lin[2], 22528); SB;
        WAITL(4); SB;
        MFMA(accH[0][4], bh_18_0_4, ah_18_0_0); SB;
        const v4u bh_19_0_1 = LDS128(lin[2], 17408); SB;
        MFMA(accH[0][4], bh_18_1_4, ah_18_1_0); SB;
        const v4u bh_19_1_1 = LDS128(lin[2], 23552); SB;
        MFMA(accH[0][5], bh_18_0_5, ah_18_0_0); SB;
        const v4u bh_19_0_2 = LDS128(lin[2], 18432); SB;
        MFMA(accH[0][5], bh_18_1_5, ah_18_1_0); SB;
        const v4u bh_19_1_2 = LDS128(lin[2], 24576); SB;
        WAITL(2); SB;
        MFMA(accH[0][0], bh_19_0_0, ah_19_0_0); SB;
        const v4u bh_19_0_3 = LDS128(lin[2], 19456); SB;
        MFMA(accH[0][0], bh_19_1_0, ah_19_1_0); SB;
        const v4u bh_19_1_3 = LDS128(lin[2], 25600); SB;
        const v4u bh_19_0_4 = LDS128(lin[2], 20480); SB;
        MFMA(accH[0][1], bh_19_0_1, ah_19_0_0); SB;
        const v4u bh_19_1_4 = LDS128(lin[2], 26624); SB;
        MFMA(accH[0][1], bh_19_1_1, ah_19_1_0); SB;
        const v4u bh_19_0_5 = LDS128(lin[2], 21504); SB;
        WAITL(3); SB;
        MFMA(accH[0][2], bh_19_0_2, ah_19_0_0); SB;
        const v4u bh_19_1_5 = LDS128(lin[2], 27648); SB;
        MFMA(accH[0][2], bh_19_1_2, ah_19_1_0); SB;
        BAR(); SB;
        const v4u bh_20_0_0 = LDS128(lin[0], 49152); SB;
        MFMA(accH[0][3], bh_19_0_3, ah_19_0_0); SB;
        const v4u bh_20_1_0 = LDS128(lin[0], 55296); SB;
        MFMA(accH[0][3], bh_19_1_3, ah_19_1_0); SB;
        const v4u bh_20_0_1 = LDS128(lin[0], 50176); SB;
        const v4u bh_20_1_1 = LDS128(lin[0], 56320); SB;
        WAITL(4); SB;
        MFMA(accH[0][4], bh_19_0_4, ah_19_0_0); SB;
        const v4u bh_20_0_2 = LDS128(lin[0], 51200); SB;
        MFMA(accH[0][4], bh_19_1_4, ah_19_1_0); SB;
        const v4u bh_20_1_2 = LDS128(lin[0], 57344); SB;
        MFMA(accH[0][5], bh_19_0_5, ah_19_0_0); SB;
        const v4u bh_20_0_3 = LDS128(lin[0], 52224); SB;
        MFMA(accH[0][5], bh_19_1_5, ah_19_1_0); SB;
        const v4u bh_20_1_3 = LDS128(lin[0], 58368); SB;
        WAITL(4); SB;
        MFMA(accH[0][0], bh_20_0_0, sa[8]); SB;
        const v4u bh_20_0_4 = LDS128(lin[0], 53248); SB;
        MFMA(accH[0][0], bh_20_1_0, sa[9]); SB;
        const v4u bh_20_1_4 = LDS128(lin[0], 59392); SB;
        MFMA(accH[0][1], bh_20_0_1, sa[8]); SB;
        const v4u bh_20_0_5 = LDS128(lin[0], 54272); SB;
        MFMA(accH[0][1], bh_20_1_1, sa[9]); SB;
        const v4u bh_20_1_5 = LDS128(lin[0], 60416); SB;
        WAITL(4); SB;
        MFMA(accH[0][2], bh_20_0_2, sa[8]); SB;
        BAR(); SB;
        const v4u ah_21_0_0 = LDS128(swz0[1], 45056); SB;
        MFMA(accH[0][2], bh_20_1_2, sa[9]); SB;
        const v4u ah_21_1_0 = LDS128(swz1[1], 45056); SB;
        MFMA(accH[0][3], bh_20_0_3, sa[8]); SB;
        const v4u bh_21_0_0 = LDS128(lin[0], 0); SB;
        MFMA(accH[0][3], bh_20_1_3, sa[9]); SB;
        const v4u bh_21_1_0 = LDS128(lin[0], 6144); SB;
        WAITL(4); SB;
        MFMA(accH[0][4], bh_20_0_4, sa[8]); SB;
        const v4u bh_21_0_1 = LDS128(lin[0], 1024); SB;
        MFMA(accH[0][4], bh_20_1_4, sa[9]); SB;
        const v4u bh_21_1_1 = LDS128(lin[0], 7168); SB;
        MFMA(accH[0][5], bh_20_0_5, sa[8]); SB;
        const v4u bh_21_0_2 = LDS128(lin[0], 2048); SB;
        MFMA(accH[0][5], bh_20_1_5, sa[9]); SB;
        const v4u bh_21_1_2 = LDS128(lin[0], 8192); SB;
        WAITL(2); SB;
        MFMA(accH[0][0], bh_21_0_0, ah_21_0_0); SB;
        const v4u bh_21_0_3 = LDS128(lin[0], 3072); SB;
        MFMA(accH[0][0], bh_21_1_0, ah_21_1_0); SB;
        const v4u bh_21_1_3 = LDS128(lin[0], 9216); SB;
        const v4u bh_21_0_4 = LDS128(lin[0], 4096); SB;
        MFMA(accH[0][1], bh_21_0_1, ah_21_0_0); SB;
        const v4u bh_21_1_4 = LDS128(lin[0], 10240); SB;
        MFMA(accH[0][1], bh_21_1_1, ah_21_1_0); SB;
        const v4u bh_21_0_5 = LDS128(lin[0], 5120); SB;
        WAITL(3); SB;
        MFMA(accH[0][2], bh_21_0_2, ah_21_0_0); SB;
        const v4u bh_21_1_5 = LDS128(lin[0], 11264); SB;
        MFMA(accH[0][2], bh_21_1_2, ah_21_1_0); SB;
        BAR(); SB;
        const v4u ah_22_0_0 = LDS128(swz0[1], 53248); SB;
        MFMA(accH[0][3], bh_21_0_3, ah_21_0_0); SB;
        const v4u ah_22_1_0 = LDS128(swz1[1], 53248); SB;
        MFMA(accH[0][3], bh_21_1_3, ah_21_1_0); SB;
        const v4u bh_22_0_0 = LDS128(lin[0], 12288); SB;
        const v4u bh_22_1_0 = LDS128(lin[0], 18432); SB;
        WAITL(4); SB;
        MFMA(accH[0][4], bh_21_0_4, ah_21_0_0); SB;
        const v4u bh_22_0_1 = LDS128(lin[0], 13312); SB;
        MFMA(accH[0][4], bh_21_1_4, ah_21_1_0); SB;
        const v4u bh_22_1_1 = LDS128(lin[0], 19456); SB;
        MFMA(accH[0][5], bh_21_0_5, ah_21_0_0); SB;
        const v4u bh_22_0_2 = LDS128(lin[0], 14336); SB;
        MFMA(accH[0][5], bh_21_1_5, ah_21_1_0); SB;
        const v4u bh_22_1_2 = LDS128(lin[0], 20480); SB;
        WAITL(2); SB;
        MFMA(accH[0][0], bh_22_0_0, ah_22_0_0); SB;
        const v4u bh_22_0_3 = LDS128(lin[0], 15360); SB;
        MFMA(accH[0][0], bh_22_1_0, ah_22_1_0); SB;
        const v4u bh_22_1_3 = LDS128(lin[0], 21504); SB;
        const v4u bh_22_0_4 = LDS128(lin[0], 16384); SB;
        MFMA(accH[0][1], bh_22_0_1, ah_22_0_0); SB;
        const v4u bh_22_1_4 = LDS128(lin[0], 22528); SB;
        MFMA(accH[0][1], bh_22_1_1, ah_22_1_0); SB;
        const v4u bh_22_0_5 = LDS128(lin[0], 17408); SB;
        WAITL(3); SB;
        MFMA(accH[0][2], bh_22_0_2, ah_22_0_0); SB;
        const v4u bh_22_1_5 = LDS128(lin[0], 23552); SB;
        MFMA(accH[0][2], bh_22_1_2, ah_22_1_0); SB;
        BAR(); SB;
        const v4u ah_23_0_0 = LDS128(swz0[1], 61440); SB;
        MFMA(accH[0][3], bh_22_0_3, ah_22_0_0); SB;
        const v4u ah_23_1_0 = LDS128(swz1[1], 61440); SB;
        MFMA(accH[0][3], bh_22_1_3, ah_22_1_0); SB;
        const v4u bh_23_0_0 = LDS128(lin[0], 24576); SB;
        const v4u bh_23_1_0 = LDS128(lin[0], 30720); SB;
        WAITL(4); SB;
        MFMA(accH[0][4], bh_22_0_4, ah_22_0_0); SB;
        const v4u bh_23_0_1 = LDS128(lin[0], 25600); SB;
        MFMA(accH[0][4], bh_22_1_4, ah_22_1_0); SB;
        const v4u bh_23_1_1 = LDS128(lin[0], 31744); SB;
        MFMA(accH[0][5], bh_22_0_5, ah_22_0_0); SB;
        const v4u bh_23_0_2 = LDS128(lin[0], 26624); SB;
        MFMA(accH[0][5], bh_22_1_5, ah_22_1_0); SB;
        const v4u bh_23_1_2 = LDS128(lin[0], 32768); SB;
        WAITL(2); SB;
        MFMA(accH[0][0], bh_23_0_0, ah_23_0_0); SB;
        const v4u bh_23_0_3 = LDS128(lin[0], 27648); SB;
        MFMA(accH[0][0], bh_23_1_0, ah_23_1_0); SB;
        const v4u bh_23_1_3 = LDS128(lin[0], 33792); SB;
        const v4u bh_23_0_4 = LDS128(lin[0], 28672); SB;
        MFMA(accH[0][1], bh_23_0_1, ah_23_0_0); SB;
        const v4u bh_23_1_4 = LDS128(lin[0], 34816); SB;
        MFMA(accH[0][1], bh_23_1_1, ah_23_1_0); SB;
        const v4u bh_23_0_5 = LDS128(lin[0], 29696); SB;
        WAITL(3); SB;
        MFMA(accH[0][2], bh_23_0_2, ah_23_0_0); SB;
        const v4u bh_23_1_5 = LDS128(lin[0], 35840); SB;
        MFMA(accH[0][2], bh_23_1_2, ah_23_1_0); SB;
        BAR(); SB;
        const v4u ah_24_0_0 = LDS128(swz0[2], 4096); SB;
        MFMA(accH[0][3], bh_23_0_3, ah_23_0_0); SB;
        const v4u ah_24_1_0 = LDS128(swz1[2], 4096); SB;
        MFMA(accH[0][3], bh_23_1_3, ah_23_1_0); SB;
        const v4u bh_24_0_0 = LDS128(lin[0], 36864); SB;
        const v4u bh_24_1_0 = LDS128(lin[0], 43008); SB;
        WAITL(4); SB;
        MFMA(accH[0][4], bh_23_0_4, ah_23_0_0); SB;
        const v4u bh_24_0_1 = LDS128(lin[0], 37888); SB;
        MFMA(accH[0][4], bh_23_1_4, ah_23_1_0); SB;
        const v4u bh_24_1_1 = LDS128(lin[0], 44032); SB;
        MFMA(accH[0][5], bh_23_0_5, ah_23_0_0); SB;
        const v4u bh_24_0_2 = LDS128(lin[0], 38912); SB;
        MFMA(accH[0][5], bh_23_1_5, ah_23_1_0); SB;
        const v4u bh_24_1_2 = LDS128(lin[0], 45056); SB;
        WAITL(2); SB;
        MFMA(accH[0][0], bh_24_0_0, ah_24_0_0); SB;
        const v4u bh_24_0_3 = LDS128(lin[0], 39936); SB;
        MFMA(accH[0][0], bh_24_1_0, ah_24_1_0); SB;
        const v4u bh_24_1_3 = LDS128(lin[0], 46080); SB;
        const v4u bh_24_0_4 = LDS128(lin[0], 40960); SB;
        MFMA(accH[0][1], bh_24_0_1, ah_24_0_0); SB;
        const v4u bh_24_1_4 = LDS128(lin[0], 47104); SB;
        MFMA(accH[0][1], bh_24_1_1, ah_24_1_0); SB;
        const v4u bh_24_0_5 = LDS128(lin[0], 41984); SB;
        WAITL(3); SB;
        MFMA(accH[0][2], bh_24_0_2, ah_24_0_0); SB;
        const v4u bh_24_1_5 = LDS128(lin[0], 48128); SB;
        MFMA(accH[0][2], bh_24_1_2, ah_24_1_0); SB;
        BAR(); SB;
        const v4u bh_25_0_0 = LDS128(lin[2], 16384); SB;
        MFMA(accH[0][3], bh_24_0_3, ah_24_0_0); SB;
        const v4u bh_25_1_0 = LDS128(lin[2], 22528); SB;
        MFMA(accH[0][3], bh_24_1_3, ah_24_1_0); SB;
        const v4u bh_25_0_1 = LDS128(lin[2], 17408); SB;
        const v4u bh_25_1_1 = LDS128(lin[2], 23552); SB;
        WAITL(4); SB;
        MFMA(accH[0][4], bh_24_0_4, ah_24_0_0); SB;
        const v4u bh_25_0_2 = LDS128(lin[2], 18432); SB;
        MFMA(accH[0][4], bh_24_1_4, ah_24_1_0); SB;
        const v4u bh_25_1_2 = LDS128(lin[2], 24576); SB;
        MFMA(accH[0][5], bh_24_0_5, ah_24_0_0); SB;
        const v4u bh_25_0_3 = LDS128(lin[2], 19456); SB;
        MFMA(accH[0][5], bh_24_1_5, ah_24_1_0); SB;
        const v4u bh_25_1_3 = LDS128(lin[2], 25600); SB;
        WAITL(4); SB;
        MFMA(accH[0][0], bh_25_0_0, sa[10]); SB;
        const v4u bh_25_0_4 = LDS128(lin[2], 20480); SB;
        MFMA(accH[0][0], bh_25_1_0, sa[11]); SB;
        const v4u bh_25_1_4 = LDS128(lin[2], 26624); SB;
        MFMA(accH[0][1], bh_25_0_1, sa[10]); SB;
        const v4u bh_25_0_5 = LDS128(lin[2], 21504); SB;
        MFMA(accH[0][1], bh_25_1_1, sa[11]); SB;
        const v4u bh_25_1_5 = LDS128(lin[2], 27648); SB;
        WAITL(4); SB;
        MFMA(accH[0][2], bh_25_0_2, sa[10]); SB;
        BAR(); SB;
        const v4u ah_26_0_0 = LDS128(swz0[0], 61440); SB;
        MFMA(accH[0][2], bh_25_1_2, sa[11]); SB;
        const v4u ah_26_1_0 = LDS128(swz1[0], 61440); SB;
        MFMA(accH[0][3], bh_25_0_3, sa[10]); SB;
        const v4u bh_26_0_0 = LDS128(lin[0], 49152); SB;
        MFMA(accH[0][3], bh_25_1_3, sa[11]); SB;
        const v4u bh_26_1_0 = LDS128(lin[0], 55296); SB;
        WAITL(4); SB;
        MFMA(accH[0][4], bh_25_0_4, sa[10]); SB;
        const v4u bh_26_0_1 = LDS128(lin[0], 50176); SB;
        MFMA(accH[0][4], bh_25_1_4, sa[11]); SB;
        const v4u bh_26_1_1 = LDS128(lin[0], 56320); SB;
        MFMA(accH[0][5], bh_25_0_5, sa[10]); SB;
        const v4u bh_26_0_2 = LDS128(lin[0], 51200); SB;
        MFMA(accH[0][5], bh_25_1_5, sa[11]); SB;
        const v4u bh_26_1_2 = LDS128(lin[0], 57344); SB;
        WAITL(2); SB;
        MFMA(accH[0][0], bh_26_0_0, ah_26_0_0); SB;
        const v4u bh_26_0_3 = LDS128(lin[0], 52224); SB;
        MFMA(accH[0][0], bh_26_1_0, ah_26_1_0); SB;
        const v4u bh_26_1_3 = LDS128(lin[0], 58368); SB;
        const v4u bh_26_0_4 = LDS128(lin[0], 53248); SB;
        MFMA(accH[0][1], bh_26_0_1, ah_26_0_0); SB;
        const v4u bh_26_1_4 = LDS128(lin[0], 59392); SB;
        MFMA(accH[0][1], bh_26_1_1, ah_26_1_0); SB;
        const v4u bh_26_0_5 = LDS128(lin[0], 54272); SB;
        WAITL(3); SB;
        MFMA(accH[0][2], bh_26_0_2, ah_26_0_0); SB;
        const v4u bh_26_1_5 = LDS128(lin[0], 60416); SB;
        MFMA(accH[0][2], bh_26_1_2, ah_26_1_0); SB;
        BAR(); SB;
        const v4u ah_27_0_0 = LDS128(swz0[1], 4096); SB;
        MFMA(accH[0][3], bh_26_0_3, ah_26_0_0); SB;
        const v4u ah_27_1_0 = LDS128(swz1[1], 4096); SB;
        MFMA(accH[0][3], bh_26_1_3, ah_26_1_0); SB;
        const v4u bh_27_0_0 = LDS128(lin[0], 0); SB;
        const v4u bh_27_1_0 = LDS128(lin[0], 6144); SB;
        WAITL(4); SB;
        MFMA(accH[0][4], bh_26_0_4, ah_26_0_0); SB;
        const v4u bh_27_0_1 = LDS128(lin[0], 1024); SB;
        MFMA(accH[0][4], bh_26_1_4, ah_26_1_0); SB;
        const v4u bh_27_1_1 = LDS128(lin[0], 7168); SB;
        MFMA(accH[0][5], bh_26_0_5, ah_26_0_0); SB;
        const v4u bh_27_0_2 = LDS128(lin[0], 2048); SB;
        MFMA(accH[0][5], bh_26_1_5, ah_26_1_0); SB;
        const v4u bh_27_1_2 = LDS128(lin[0], 8192); SB;
        WAITL(2); SB;
        MFMA(accH[0][0], bh_27_0_0, ah_27_0_0); SB;
        const v4u bh_27_0_3 = LDS128(lin[0], 3072); SB;
        MFMA(accH[0][0], bh_27_1_0, ah_27_1_0); SB;
        const v4u bh_27_1_3 = LDS128(lin[0], 9216); SB;
        const v4u bh_27_0_4 = LDS128(lin[0], 4096); SB;
        MFMA(accH[0][1], bh_27_0_1, ah_27_0_0); SB;
        const v4u bh_27_1_4 = LDS128(lin[0], 10240); SB;
        MFMA(accH[0][1], bh_27_1_1, ah_27_1_0); SB;
        const v4u bh_27_0_5 = LDS128(lin[0], 5120); SB;
        WAITL(3); SB;
        MFMA(accH[0][2], bh_27_0_2, ah_27_0_0); SB;
        const v4u bh_27_1_5 = LDS128(lin[0], 11264); SB;
        MFMA(accH[0][2], bh_27_1_2, ah_27_1_0); SB;
        BAR(); SB;
        const v4u ah_28_0_0 = LDS128(swz0[1], 12288); SB;
        MFMA(accH[0][3], bh_27_0_3, ah_27_0_0); SB;
        const v4u ah_28_1_0 = LDS128(swz1[1], 12288); SB;
        MFMA(accH[0][3], bh_27_1_3, ah_27_1_0); SB;
        const v4u bh_28_0_0 = LDS128(lin[0], 12288); SB;
        const v4u bh_28_1_0 = LDS128(lin[0], 18432); SB;
        WAITL(4); SB;
        MFMA(accH[0][4], bh_27_0_4, ah_27_0_0); SB;
        const v4u bh_28_0_1 = LDS128(lin[0], 13312); SB;
        MFMA(accH[0][4], bh_27_1_4, ah_27_1_0); SB;
        const v4u bh_28_1_1 = LDS128(lin[0], 19456); SB;
        MFMA(accH[0][5], bh_27_0_5, ah_27_0_0); SB;
        const v4u bh_28_0_2 = LDS128(lin[0], 14336); SB;
        MFMA(accH[0][5], bh_27_1_5, ah_27_1_0); SB;
        const v4u bh_28_1_2 = LDS128(lin[0], 20480); SB;
        WAITL(2); SB;
        MFMA(accH[0][0], bh_28_0_0, ah_28_0_0); SB;
        const v4u bh_28_0_3 = LDS128(lin[0], 15360); SB;
        MFMA(accH[0][0], bh_28_1_0, ah_28_1_0); SB;
        const v4u bh_28_1_3 = LDS128(lin[0], 21504); SB;
        const v4u bh_28_0_4 = LDS128(lin[0], 16384); SB;
        MFMA(accH[0][1], bh_28_0_1, ah_28_0_0); SB;
        const v4u bh_28_1_4 = LDS128(lin[0], 22528); SB;
        MFMA(accH[0][1], bh_28_1_1, ah_28_1_0); SB;
        const v4u bh_28_0_5 = LDS128(lin[0], 17408); SB;
        WAITL(3); SB;
        MFMA(accH[0][2], bh_28_0_2, ah_28_0_0); SB;
        const v4u bh_28_1_5 = LDS128(lin[0], 23552); SB;
        MFMA(accH[0][2], bh_28_1_2, ah_28_1_0); SB;
        BAR(); SB;
        const v4u bh_29_0_0 = LDS128(lin[0], 24576); SB;
        MFMA(accH[0][3], bh_28_0_3, ah_28_0_0); SB;
        const v4u bh_29_1_0 = LDS128(lin[0], 30720); SB;
        MFMA(accH[0][3], bh_28_1_3, ah_28_1_0); SB;
        const v4u bh_29_0_1 = LDS128(lin[0], 25600); SB;
        const v4u bh_29_1_1 = LDS128(lin[0], 31744); SB;
        WAITL(4); SB;
        MFMA(accH[0][4], bh_28_0_4, ah_28_0_0); SB;
        const v4u bh_29_0_2 = LDS128(lin[0], 26624); SB;
        MFMA(accH[0][4], bh_28_1_4, ah_28_1_0); SB;
        const v4u bh_29_1_2 = LDS128(lin[0], 32768); SB;
        MFMA(accH[0][5], bh_28_0_5, ah_28_0_0); SB;
        const v4u bh_29_0_3 = LDS128(lin[0], 27648); SB;
        MFMA(accH[0][5], bh_28_1_5, ah_28_1_0); SB;
        const v4u bh_29_1_3 = LDS128(lin[0], 33792); SB;
        WAITL(4); SB;
        MFMA(accH[0][0], bh_29_0_0, sa[12]); SB;
        const v4u bh_29_0_4 = LDS128(lin[0], 28672); SB;
        MFMA(accH[0][0], bh_29_1_0, sa[13]); SB;
        const v4u bh_29_1_4 = LDS128(lin[0], 34816); SB;
        MFMA(accH[0][1], bh_29_0_1, sa[12]); SB;
        const v4u bh_29_0_5 = LDS128(lin[0], 29696); SB;
        MFMA(accH[0][1], bh_29_1_1, sa[13]); SB;
        const v4u bh_29_1_5 = LDS128(lin[0], 35840); SB;
        WAITL(4); SB;
        MFMA(accH[0][2], bh_29_0_2, sa[12]); SB;
        BAR(); SB;
        const v4u ah_30_0_0 = LDS128(swz0[1], 20480); SB;
        MFMA(accH[0][2], bh_29_1_2, sa[13]); SB;
        const v4u ah_30_1_0 = LDS128(swz1[1], 20480); SB;
        MFMA(accH[0][3], bh_29_0_3, sa[12]); SB;
        const v4u bh_30_0_0 = LDS128(lin[0], 36864); SB;
        MFMA(accH[0][3], bh_29_1_3, sa[13]); SB;
        const v4u bh_30_1_0 = LDS128(lin[0], 43008); SB;
        WAITL(4); SB;
        MFMA(accH[0][4], bh_29_0_4, sa[12]); SB;
        const v4u bh_30_0_1 = LDS128(lin[0], 37888); SB;
        MFMA(accH[0][4], bh_29_1_4, sa[13]); SB;
        const v4u bh_30_1_1 = LDS128(lin[0], 44032); SB;
        MFMA(accH[0][5], bh_29_0_5, sa[12]); SB;
        const v4u bh_30_0_2 = LDS128(lin[0], 38912); SB;
        MFMA(accH[0][5], bh_29_1_5, sa[13]); SB;
        const v4u bh_30_1_2 = LDS128(lin[0], 45056); SB;
        WAITL(2); SB;
        MFMA(accH[0][0], bh_30_0_0, ah_30_0_0); SB;
        const v4u bh_30_0_3 = LDS128(lin[0], 39936); SB;
        MFMA(accH[0][0], bh_30_1_0, ah_30_1_0); SB;
        const v4u bh_30_1_3 = LDS128(lin[0], 46080); SB;
        const v4u bh_30_0_4 = LDS128(lin[0], 40960); SB;
        MFMA(accH[0][1], bh_30_0_1, ah_30_0_0); SB;
        const v4u bh_30_1_4 = LDS128(lin[0], 47104); SB;
        MFMA(accH[0][1], bh_30_1_1, ah_30_1_0); SB;
        const v4u bh_30_0_5 = LDS128(lin[0], 41984); SB;
        WAITL(3); SB;
        MFMA(accH[0][2], bh_30_0_2, ah_30_0_0); SB;
        const v4u bh_30_1_5 = LDS128(lin[0], 48128); SB;
        MFMA(accH[0][2], bh_30_1_2, ah_30_1_0); SB;
        BAR(); SB;
        const v4u ah_31_0_0 = LDS128(swz0[1], 28672); SB;
        MFMA(accH[0][3], bh_30_0_3, ah_30_0_0); SB;
        const v4u ah_31_1_0 = LDS128(swz1[1], 28672); SB;
        MFMA(accH[0][3], bh_30_1_3, ah_30_1_0); SB;
        const v4u bh_31_0_0 = LDS128(lin[2], 16384); SB;
        const v4u bh_31_1_0 = LDS128(lin[2], 22528); SB;
        WAITL(4); SB;
        MFMA(accH[0][4], bh_30_0_4, ah_30_0_0); SB;
        const v4u bh_31_0_1 = LDS128(lin[2], 17408); SB;
        MFMA(accH[0][4], bh_30_1_4, ah_30_1_0); SB;
        const v4u bh_31_1_1 = LDS128(lin[2], 23552); SB;
        MFMA(accH[0][5], bh_30_0_5, ah_30_0_0); SB;
        const v4u bh_31_0_2 = LDS128(lin[2], 18432); SB;
        MFMA(accH[0][5], bh_30_1_5, ah_30_1_0); SB;
        const v4u bh_31_1_2 = LDS128(lin[2], 24576); SB;
        WAITL(2); SB;
        MFMA(accH[0][0], bh_31_0_0, ah_31_0_0); SB;
        const v4u bh_31_0_3 = LDS128(lin[2], 19456); SB;
        MFMA(accH[0][0], bh_31_1_0, ah_31_1_0); SB;
        const v4u bh_31_1_3 = LDS128(lin[2], 25600); SB;
        const v4u bh_31_0_4 = LDS128(lin[2], 20480); SB;
        MFMA(accH[0][1], bh_31_0_1, ah_31_0_0); SB;
        const v4u bh_31_1_4 = LDS128(lin[2], 26624); SB;
        MFMA(accH[0][1], bh_31_1_1, ah_31_1_0); SB;
        const v4u bh_31_0_5 = LDS128(lin[2], 21504); SB;
        WAITL(3); SB;
        MFMA(accH[0][2], bh_31_0_2, ah_31_0_0); SB;
        const v4u bh_31_1_5 = LDS128(lin[2], 27648); SB;
        MFMA(accH[0][2], bh_31_1_2, ah_31_1_0); SB;
        MFMA(accH[0][3], bh_31_0_3, ah_31_0_0); SB;
        MFMA(accH[0][3], bh_31_1_3, ah_31_1_0); SB;
        WAITL(0); SB;
        MFMA(accH[0][4], bh_31_0_4, ah_31_0_0); SB;
        MFMA(accH[0][4], bh_31_1_4, ah_31_1_0); SB;
        MFMA(accH[0][5], bh_31_0_5, ah_31_0_0); SB;
        MFMA(accH[0][5], bh_31_1_5, ah_31_1_0); SB;
        MFMA_END;
        C_EPI_H();
        NEW_LIN();
        BAR(); SB;
        INIT5(accO, hk0, hk1, hk2);
        const v4u wo_0 = LDS128(lin[1], 36864); SB;
        const v4u wo_1 = LDS128(lin[1], 41984); SB;
        const v4u wo_2 = LDS128(lin[1], 47104); SB;
        const v4u wo_3 = LDS128(lin[1], 37888); SB;
        const v4u wo_4 = LDS128(lin[1], 43008); SB;
        const v4u wo_5 = LDS128(lin[1], 48128); SB;
        const v4u wo_6 = LDS128(lin[1], 38912); SB;
        const v4u wo_7 = LDS128(lin[1], 44032); SB;
        const v4u wo_8 = LDS128(lin[1], 49152); SB;
        WAITL(7); SB;
        MFMA(accO[0], wo_0, uf[0]); SB;
        const v4u wo_9 = LDS128(lin[1], 39936); SB;
        MFMA(accO[0], wo_1, uf[1]); SB;
        const v4u wo_10 = LDS128(lin[1], 45056); SB;
        WAITL(7); SB;
        MFMA(accO[0], wo_2, uf[2]); SB;
        const v4u wo_11 = LDS128(lin[1], 50176); SB;
        MFMA(accO[1], wo_3, uf[0]); SB;
        const v4u wo_12 = LDS128(lin[1], 40960); SB;
        WAITL(7); SB;
        MFMA(accO[1], wo_4, uf[1]); SB;
        const v4u wo_13 = LDS128(lin[1], 46080); SB;
        MFMA(accO[1], wo_5, uf[2]); SB;
        const v4u wo_14 = LDS128(lin[1], 51200); SB;
        WAITL(7); SB;
        MFMA(accO[2], wo_6, uf[0]); SB;
        MFMA(accO[2], wo_7, uf[1]); SB;
        WAITL(5); SB;
        MFMA(accO[2], wo_8, uf[2]); SB;
        MFMA(accO[3], wo_9, uf[0]); SB;
        WAITL(3); SB;
        MFMA(accO[3], wo_10, uf[1]); SB;
        MFMA(accO[3], wo_11, uf[2]); SB;
        WAITL(1); SB;
        MFMA(accO[4], wo_12, uf[0]); SB;
        MFMA(accO[4], wo_13, uf[1]); SB;
        WAITL(0); SB;
        MFMA(accO[4], wo_14, uf[2]); SB;
        DRAIN();
        FLAG_PW(F5_IN);
        NEW_LIN();
        BAR(); SB;
        const v4u wo_15 = LDS128(lin[1], 52224); SB;
        const v4u wo_16 = LDS128(lin[1], 57344); SB;
        const v4u wo_17 = LDS128(lin[1], 53248); SB;
        const v4u wo_18 = LDS128(lin[1], 58368); SB;
        const v4u wo_19 = LDS128(lin[1], 54272); SB;
        const v4u wo_20 = LDS128(lin[1], 59392); SB;
        const v4u wo_21 = LDS128(lin[1], 55296); SB;
        const v4u wo_22 = LDS128(lin[1], 60416); SB;
        const v4u wo_23 = LDS128(lin[1], 56320); SB;
        PRE_O();
        __builtin_amdgcn_s_sleep(8);
        POLL_PW(F5_IN);
        uf[3] = LDG(vlin + 0, sU + spread(ord(1, cO) * 4096u)); SB;
        uf[4] = LDG(vlin + 1024, sU + spread(ord(1, cO) * 4096u)); SB;
        uf[5] = LDG(vlin + 2048, sU + spread(ord(1, cO) * 4096u)); SB;
        uf[6] = LDG(vlin + 0, sU + spread(ord(2, cO) * 4096u)); SB;
        uf[7] = LDG(vlin + 1024, sU + spread(ord(2, cO) * 4096u)); SB;
        uf[8] = LDG(vlin + 2048, sU + spread(ord(2, cO) * 4096u)); SB;
        uf[9] = LDG(vlin + 0, sU + spread(ord(3, cO) * 4096u)); SB;
        uf[10] = LDG(vlin + 1024, sU + spread(ord(3, cO) * 4096u)); SB;
        uf[11] = LDG(vlin + 2048, sU + spread(ord(3, cO) * 4096u)); SB;
        uf[12] = LDG(vlin + 0, sU + spread(ord(4, cO) * 4096u)); SB;
        uf[13] = LDG(vlin + 1024, sU + spread(ord(4, cO) * 4096u)); SB;
        uf[14] = LDG(vlin + 2048, sU + spread(ord(4, cO) * 4096u)); SB;
        WAITL(7); SB;
        MFMA(accO[0], wo_15, uf[3]); SB;
        const v4u wo_24 = LDS128(lin[1], 61440); SB;
        MFMA(accO[0], wo_16, uf[4]); SB;
        const v4u wo_25 = LDS128(lin[1], 62464); SB;
        WAITL(7); SB;
        MFMA(accO[1], wo_17, uf[3]); SB;
        const v4u wo_26 = LDS128(lin[2], 2048); SB;
        MFMA(accO[1], wo_18, uf[4]); SB;
        const v4u wo_27 = LDS128(lin[1], 63488); SB;
        WAITL(7); SB;
        MFMA(accO[2], wo_19, uf[3]); SB;
        uf[15] = LDG(vlin + 0, sU + spread(ord(5, cO) * 4096u)); SB;
        const v4u wo_28 = LDS128(lin[2], 3072); SB;
        MFMA(accO[2], wo_20, uf[4]); SB;
        const v4u wo_29 = LDS128(lin[1], 64512); SB;
        WAITL(7); SB;
        MFMA(accO[3], wo_21, uf[3]); SB;
        const v4u wo_30 = LDS128(lin[2], 4096); SB;
        MFMA(accO[3], wo_22, uf[4]); SB;
        const v4u wo_31 = LDS128(lin[2], 0); SB;
        WAITL(7); SB;
        MFMA(accO[4], wo_23, uf[3]); SB;
        const v4u wo_32 = LDS128(lin[2], 5120); SB;
        MFMA(accO[4], wo_24, uf[4]); SB;
        uf[16] = LDG(vlin + 1024, sU + spread(ord(5, cO) * 4096u)); SB;
        const v4u wo_33 = LDS128(lin[2], 1024); SB;
        WAITL(7); SB;
        MFMA(accO[0], wo_25, uf[5]); SB;
        const v4u wo_34 = LDS128(lin[2], 6144); SB;
        MFMA(accO[0], wo_26, uf[6]); SB;
        const v4u wo_35 = LDS128(lin[2], 7168); SB;
        WAITL(7); SB;
        MFMA(accO[1], wo_27, uf[5]); SB;
        const v4u wo_36 = LDS128(lin[0], 49152); SB;
        MFMA(accO[1], wo_28, uf[6]); SB;
        const v4u wo_37 = LDS128(lin[2], 8192); SB;
        WAITL(7); SB;
        MFMA(accO[2], wo_29, uf[5]); SB;
        uf[17] = LDG(vlin + 2048, sU + spread(ord(5, cO) * 4096u)); SB;
        const v4u wo_38 = LDS128(lin[0], 50176); SB;
        MFMA(accO[2], wo_30, uf[6]); SB;
        const v4u wo_39 = LDS128(lin[2], 9216); SB;
        WAITL(7); SB;
        MFMA(accO[3], wo_31, uf[5]); SB;
        const v4u wo_40 = LDS128(lin[0], 51200); SB;
        MFMA(accO[3], wo_32, uf[6]); SB;
        const v4u wo_41 = LDS128(lin[2], 10240); SB;
        WAITL(7); SB;
        MFMA(accO[4], wo_33, uf[5]); SB;
        const v4u wo_42 = LDS128(lin[0], 52224); SB;
        MFMA(accO[4], wo_34, uf[6]); SB;
        uf[18] = LDG(vlin + 0, sU + spread(ord(6, cO) * 4096u)); SB;
        const v4u wo_43 = LDS128(lin[2], 11264); SB;
        WAITL(7); SB;
        MFMA(accO[0], wo_35, uf[7]); SB;
        const v4u wo_44 = LDS128(lin[0], 53248); SB;
        MFMA(accO[0], wo_36, uf[8]); SB;
        const v4u wo_45 = LDS128(lin[0], 54272); SB;
        WAITL(7); SB;
        MFMA(accO[1], wo_37, uf[7]); SB;
        const v4u wo_46 = LDS128(lin[0], 59392); SB;
        MFMA(accO[1], wo_38, uf[8]); SB;
        const v4u wo_47 = LDS128(lin[0], 55296); SB;
        WAITL(7); SB;
        MFMA(accO[2], wo_39, uf[7]); SB;
        uf[19] = LDG(vlin + 1024, sU + spread(ord(6, cO) * 4096u)); SB;
        const v4u wo_48 = LDS128(lin[0], 60416); SB;
        MFMA(accO[2], wo_40, uf[8]); SB;
        const v4u wo_49 = LDS128(lin[0], 56320); SB;
        WAITL(7); SB;
        MFMA(accO[3], wo_41, uf[7]); SB;
        const v4u wo_50 = LDS128(lin[0], 61440); SB;
        MFMA(accO[3], wo_42, uf[8]); SB;
        const v4u wo_51 = LDS128(lin[0], 57344); SB;
        WAITL(7); SB;
        MFMA(accO[4], wo_43, uf[7]); SB;
        const v4u wo_52 = LDS128(lin[0], 62464); SB;
        MFMA(accO[4], wo_44, uf[8]); SB;
        uf[20] = LDG(vlin + 2048, sU + spread(ord(6, cO) * 4096u)); SB;
        const v4u wo_53 = LDS128(lin[0], 58368); SB;
        WAITL(7); SB;
        MFMA(accO[0], wo_45, uf[9]); SB;
        const v4u wo_54 = LDS128(lin[0], 63488); SB;
        MFMA(accO[0], wo_46, uf[10]); SB;
        BAR(); SB;
        const v4u wo_55 = LDS128(lin[0], 64512); SB;
        WAITL(7); SB;
        MFMA(accO[1], wo_47, uf[9]); SB;
        const v4u wo_56 = LDS128(lin[0], 0); SB;
        MFMA(accO[1], wo_48, uf[10]); SB;
        const v4u wo_57 = LDS128(lin[1], 0); SB;
        WAITL(7); SB;
        MFMA(accO[2], wo_49, uf[9]); SB;
        uf[21] = LDG(vlin + 0, sU + spread(ord(7, cO) * 4096u)); SB;
        const v4u wo_58 = LDS128(lin[0], 1024); SB;
        MFMA(accO[2], wo_50, uf[10]); SB;
        const v4u wo_59 = LDS128(lin[1], 1024); SB;
        WAITL(7); SB;
        MFMA(accO[3], wo_51, uf[9]); SB;
        const v4u wo_60 = LDS128(lin[0], 2048); SB;
        MFMA(accO[3], wo_52, uf[10]); SB;
        const v4u wo_61 = LDS128(lin[1], 2048); SB;
        WAITL(7); SB;
        MFMA(accO[4], wo_53, uf[9]); SB;
        const v4u wo_62 = LDS128(lin[0], 3072); SB;
        MFMA(accO[4], wo_54, uf[10]); SB;
        uf[22] = LDG(vlin + 1024, sU + spread(ord(7, cO) * 4096u)); SB;
        const v4u wo_63 = LDS128(lin[1], 3072); SB;
        WAITL(7); SB;
        MFMA(accO[0], wo_55, uf[11]); SB;
        const v4u wo_64 = LDS128(lin[0], 4096); SB;
        MFMA(accO[0], wo_56, uf[12]); SB;
        const v4u wo_65 = LDS128(lin[0], 5120); SB;
        WAITL(7); SB;
        MFMA(accO[1], wo_57, uf[11]); SB;
        const v4u wo_66 = LDS128(lin[0], 10240); SB;
        MFMA(accO[1], wo_58, uf[12]); SB;
        const v4u wo_67 = LDS128(lin[0], 6144); SB;
        WAITL(7); SB;
        MFMA(accO[2], wo_59, uf[11]); SB;
        uf[23] = LDG(vlin + 2048, sU + spread(ord(7, cO) * 4096u)); SB;
        const v4u wo_68 = LDS128(lin[0], 11264); SB;
        MFMA(accO[2], wo_60, uf[12]); SB;
        const v4u wo_69 = LDS128(lin[0], 7168); SB;
        WAITL(7); SB;
        MFMA(accO[3], wo_61, uf[11]); SB;
        const v4u wo_70 = LDS128(lin[0], 12288); SB;
        MFMA(accO[3], wo_62, uf[12]); SB;
        const v4u wo_71 = LDS128(lin[0], 8192); SB;
        WAITL(7); SB;
        MFMA(accO[4], wo_63, uf[11]); SB;
        const v4u wo_72 = LDS128(lin[0], 13312); SB;
        MFMA(accO[4], wo_64, uf[12]); SB;
        const v4u wo_73 = LDS128(lin[0], 9216); SB;
        WAITL(7); SB;
        MFMA(accO[0], wo_65, uf[13]); SB;
        const v4u wo_74 = LDS128(lin[0], 14336); SB;
        MFMA(accO[0], wo_66, uf[14]); SB;
        const v4u wo_75 = LDS128(lin[0], 15360); SB;
        WAITL(7); SB;
        MFMA(accO[1], wo_67, uf[13]); SB;
        const v4u wo_76 = LDS128(lin[0], 20480); SB;
        MFMA(accO[1], wo_68, uf[14]); SB;
        const v4u wo_77 = LDS128(lin[0], 16384); SB;
        WAITL(7); SB;
        MFMA(accO[2], wo_69, uf[13]); SB;
        const v4u wo_78 = LDS128(lin[0], 21504); SB;
        MFMA(accO[2], wo_70, uf[14]); SB;
        const v4u wo_79 = LDS128(lin[0], 17408); SB;
        WAITL(7); SB;
        MFMA(accO[3], wo_71, uf[13]); SB;
        const v4u wo_80 = LDS128(lin[0], 22528); SB;
        MFMA(accO[3], wo_72, uf[14]); SB;
        const v4u wo_81 = LDS128(lin[0], 18432); SB;
        WAITL(7); SB;
        MFMA(accO[4], wo_73, uf[13]); SB;
        const v4u wo_82 = LDS128(lin[0], 23552); SB;
        MFMA(accO[4], wo_74, uf[14]); SB;
        const v4u wo_83 = LDS128(lin[0], 19456); SB;
        WAITL(7); SB;
        MFMA(accO[0], wo_75, uf[15]); SB;
        const v4u wo_84 = LDS128(lin[0], 24576); SB;
        MFMA(accO[0], wo_76, uf[16]); SB;
        const v4u wo_85 = LDS128(lin[0], 25600); SB;
        WAITL(7); SB;
        MFMA(accO[1], wo_77, uf[15]); SB;
        const v4u wo_86 = LDS128(lin[0], 30720); SB;
        MFMA(accO[1], wo_78, uf[16]); SB;
        const v4u wo_87 = LDS128(lin[0], 26624); SB;
        WAITL(7); SB;
        MFMA(accO[2], wo_79, uf[15]); SB;
        const v4u wo_88 = LDS128(lin[0], 31744); SB;
        MFMA(accO[2], wo_80, uf[16]); SB;
        const v4u wo_89 = LDS128(lin[0], 27648); SB;
        WAITL(7); SB;
        MFMA(accO[3], wo_81, uf[15]); SB;
        const v4u wo_90 = LDS128(lin[0], 32768); SB;
        MFMA(accO[3], wo_82, uf[16]); SB;
        const v4u wo_91 = LDS128(lin[0], 28672); SB;
        WAITL(7); SB;
        MFMA(accO[4], wo_83, uf[15]); SB;
        const v4u wo_92 = LDS128(lin[0], 33792); SB;
        MFMA(accO[4], wo_84, uf[16]); SB;
        const v4u wo_93 = LDS128(lin[0], 29696); SB;
        WAITL(7); SB;
        MFMA(accO[0], wo_85, uf[17]); SB;
        const v4u wo_94 = LDS128(lin[0], 34816); SB;
        MFMA(accO[0], wo_86, uf[18]); SB;
        const v4u wo_95 = LDS128(lin[0], 35840); SB;
        WAITL(7); SB;
        MFMA(accO[1], wo_87, uf[17]); SB;
        const v4u wo_96 = LDS128(lin[1], 4096); SB;
        MFMA(accO[1], wo_88, uf[18]); SB;
        const v4u wo_97 = LDS128(lin[0], 36864); SB;
        WAITL(7); SB;
        MFMA(accO[2], wo_89, uf[17]); SB;
        const v4u wo_98 = LDS128(lin[1], 5120); SB;
        MFMA(accO[2], wo_90, uf[18]); SB;
        const v4u wo_99 = LDS128(lin[0], 37888); SB;
        WAITL(7); SB;
        MFMA(accO[3], wo_91, uf[17]); SB;
        const v4u wo_100 = LDS128(lin[1], 6144); SB;
        MFMA(accO[3], wo_92, uf[18]); SB;
        const v4u wo_101 = LDS128(lin[0], 38912); SB;
        WAITL(7); SB;
        MFMA(accO[4], wo_93, uf[17]); SB;
        const v4u wo_102 = LDS128(lin[1], 7168); SB;
        MFMA(accO[4], wo_94, uf[18]); SB;
        const v4u wo_103 = LDS128(lin[0], 39936); SB;
        WAITL(7); SB;
        MFMA(accO[0], wo_95, uf[19]); SB;
        const v4u wo_104 = LDS128(lin[1], 8192); SB;
        MFMA(accO[0], wo_96, uf[20]); SB;
        const v4u wo_105 = LDS128(lin[1], 9216); SB;
        WAITL(7); SB;
        MFMA(accO[1], wo_97, uf[19]); SB;
        const v4u wo_106 = LDS128(lin[1], 14336); SB;
        MFMA(accO[1], wo_98, uf[20]); SB;
        const v4u wo_107 = LDS128(lin[1], 10240); SB;
        WAITL(7); SB;
        MFMA(accO[2], wo_99, uf[19]); SB;
        const v4u wo_108 = LDS128(lin[1], 15360); SB;
        MFMA(accO[2], wo_100, uf[20]); SB;
        const v4u wo_109 = LDS128(lin[1], 11264); SB;
        WAITL(7); SB;
        MFMA(accO[3], wo_101, uf[19]); SB;
        const v4u wo_110 = LDS128(lin[1], 16384); SB;
        MFMA(accO[3], wo_102, uf[20]); SB;
        const v4u wo_111 = LDS128(lin[1], 12288); SB;
        WAITL(7); SB;
        MFMA(accO[4], wo_103, uf[19]); SB;
        const v4u wo_112 = LDS128(lin[1], 17408); SB;
        MFMA(accO[4], wo_104, uf[20]); SB;
        const v4u wo_113 = LDS128(lin[1], 13312); SB;
        WAITL(7); SB;
        MFMA(accO[0], wo_105, uf[21]); SB;
        const v4u wo_114 = LDS128(lin[1], 18432); SB;
        MFMA(accO[0], wo_106, uf[22]); SB;
        const v4u wo_115 = LDS128(lin[1], 19456); SB;
        WAITL(7); SB;
        MFMA(accO[1], wo_107, uf[21]); SB;
        const v4u wo_116 = LDS128(lin[1], 20480); SB;
        MFMA(accO[1], wo_108, uf[22]); SB;
        const v4u wo_117 = LDS128(lin[1], 21504); SB;
        WAITL(7); SB;
        MFMA(accO[2], wo_109, uf[21]); SB;
        const v4u wo_118 = LDS128(lin[1], 22528); SB;
        MFMA(accO[2], wo_110, uf[22]); SB;
        const v4u wo_119 = LDS128(lin[1], 23552); SB;
        WAITL(7); SB;
        MFMA(accO[3], wo_111, uf[21]); SB;
        MFMA(accO[3], wo_112, uf[22]); SB;
        WAITL(5); SB;
        MFMA(accO[4], wo_113, uf[21]); SB;
        MFMA(accO[4], wo_114, uf[22]); SB;
        WAITL(3); SB;
        MFMA(accO[0], wo_115, uf[23]); SB;
        MFMA(accO[1], wo_116, uf[23]); SB;
        WAITL(1); SB;
        MFMA(accO[2], wo_117, uf[23]); SB;
        MFMA(accO[3], wo_118, uf[23]); SB;
        WAITL(0); SB;
        MFMA(accO[4], wo_119, uf[23]); SB;
        MFMA_END;
        C_EPI_O();
        asm volatile("s_endpgm\n .fill 762, 4, 0xBF800000" ::: "memory");
    }
}
}

extern "C" void kernel_launch(void* const* d_in, const int* in_sizes, int n_in, void* d_out, int out_size, void* d_ws, size_t ws_size, hipStream_t stream) {
    (void)in_sizes; (void)n_in; (void)out_size; (void)ws_size;
    (void)hipFuncSetAttribute((const void*)fnet_gmlp_fused, hipFuncAttributeMaxDynamicSharedMemorySize, 160 * 1024);
    fnet_gmlp_fused<<<256, 512, 160 * 1024, stream>>>((const u16*)d_in[0], (char*)d_ws, (const u16*)d_in[6], (const u16*)d_in[1], (const u16*)d_in[2], (const u16*)d_in[3],
        (const u16*)d_in[4], (const u16*)d_in[7], (const u16*)d_in[8], (const u16*)d_in[9], (u16*)d_out);
}
```
